```python
import torch

def _softmax(x, axis=-1):
    return torch.softmax(x, dim=axis)
def _gelu(x):
    return torch.nn.functional.gelu(x, approximate="tanh")
def _silu(x):
    return torch.nn.functional.silu(x)
def _sigmoid(x):
    return torch.sigmoid(x)
def _layernorm(x, axis=-1, eps=1e-5):
    m = x.mean(dim=axis, keepdim=True)
    v = x.var(dim=axis, keepdim=True, unbiased=False)
    return (x - m) / torch.sqrt(v + eps)
def _scatter_add(tgt, idx, src, axis=0):
    return tgt.clone().index_add_(axis, idx, src)
def _segment_sum(src, seg, n):
    out = torch.zeros((n,) + src.shape[1:], dtype=src.dtype)
    return out.index_add_(0, seg, src)
def _pad(x, pw):
    flat = tuple(v for pair in reversed(pw) for v in pair)
    return torch.nn.functional.pad(x, flat)

kernel_name = 'interleave_layers__softmax_full_mha__moe_arch_mod__prompt'

def setup_inputs():

    _w = (lambda a: (lambda t: t.to(torch.bfloat16) if t.is_floating_point() else t)(torch.as_tensor(a)))
    def _A_setup():
        B, S, D = 1, 2048, 512
        return {
            "x": _w((lambda *s: torch.randn(*s, dtype=torch.bfloat16) * 0.05)(B, S, D)),
            "wqkv": _w((lambda *s: torch.randn(*s, dtype=torch.bfloat16) * 0.05)(D, 3 * D)),
            "wo": _w((lambda *s: torch.randn(*s, dtype=torch.bfloat16) * 0.05)(D, D)),
        }
    _A_in = _A_setup()

    def _B_setup():
        B, S, D, F, C = 1, 2048, 512, 1792, 1
        return {
            "x": _w((lambda *s: torch.randn(*s, dtype=torch.bfloat16) * 0.05)(B, S, D)),
            "w_r": _w((lambda *s: torch.randn(*s, dtype=torch.bfloat16) * 0.05)(D)),
            "w_gate": _w((lambda *s: torch.randn(*s, dtype=torch.bfloat16) * 0.05)(D, F)),
            "w_up": _w((lambda *s: torch.randn(*s, dtype=torch.bfloat16) * 0.05)(D, F)),
            "w_down": _w((lambda *s: torch.randn(*s, dtype=torch.bfloat16) * 0.05)(F, D)),
        }
    _B_in = _B_setup()

    return {
        "A_x": _A_in["x"],
        "A_wqkv": _A_in["wqkv"],
        "A_wo": _A_in["wo"],
        "B_x": _B_in["x"],
        "B_w_r": _B_in["w_r"],
        "B_w_gate": _B_in["w_gate"],
        "B_w_up": _B_in["w_up"],
        "B_w_down": _B_in["w_down"],
    }

def reference(A_x, A_wqkv, A_wo, B_x, B_w_r, B_w_gate, B_w_up, B_w_down):
    def _A_ref(x, wqkv, wo):
        B, S, D = x.shape
        H = 32; Dh = D // H
        qkv = (lambda x, *d: x.permute(*d))((x @ wqkv).reshape(B, S, 3, H, Dh), 2, 0, 3, 1, 4)
        q, k, v = qkv[0], qkv[1], qkv[2]
        scale = Dh ** -0.5
        scores = torch.einsum("bhqd,bhkd->bhqk", q, k) * scale
        p = _softmax(scores, axis=-1)
        o = torch.einsum("bhqk,bhkd->bhqd", p, v)
        return (lambda x, *d: x.permute(*d))(o, 0, 2, 1, 3).reshape(B, S, D) @ wo

    def _B_ref(x, w_r, w_gate, w_up, w_down):
        B, S, D = x.shape
        C = 1
        r = x @ w_r
        idx = torch.argsort(-r, axis=-1)[:, :C]
        idx_e = (lambda x, n, axis: torch.repeat_interleave(x, n, dim=axis))(idx.reshape(B, C, 1), D, axis=2)
        x_sel = (lambda x, i, axis: torch.take_along_dim(x, i, dim=axis))(x, idx_e, axis=1)
        g = _silu(x_sel @ w_gate)
        u = x_sel @ w_up
        y = (g * u) @ w_down
        r_sel = _sigmoid((lambda x, i, axis: torch.take_along_dim(x, i, dim=axis))(r, idx, axis=1))
        y = y * r_sel.reshape(B, C, 1)
        b_off = (torch.arange(B) * S).reshape(B, 1)
        flat_idx = (idx + b_off).reshape(B * C)
        zero = (x * 0.0).reshape(B * S, D)
        upd = _scatter_add(zero, flat_idx, y.reshape(B * C, D), axis=0)
        return x + upd.reshape(B, S, D)

    x = A_x
    x = x + _A_ref(_layernorm(x, axis=-1), A_wqkv, A_wo)
    x = x + _B_ref(_layernorm(x, axis=-1), B_w_r, B_w_gate, B_w_up, B_w_down)
    x = x + _A_ref(_layernorm(x, axis=-1), A_wqkv, A_wo)
    x = x + _B_ref(_layernorm(x, axis=-1), B_w_r, B_w_gate, B_w_up, B_w_down)
    return x
```

```cpp
#include <hip/hip_runtime.h>
#include <hip/hip_bf16.h>
#include <stdint.h>
typedef unsigned short u16;
__device__ __forceinline__ float bf2f(u16 v) { return __uint_as_float(((unsigned)v) << 16); }
__device__ __forceinline__ u16 f2bf(float f) { unsigned u = __float_as_uint(f); u += 0x7fffu + ((u >> 16) & 1u); return (u16)(u >> 16); }
constexpr int S = 2048, D = 512, F = 1792, H = 32, DH = 16;

__global__ void k_ln(const u16* x, u16* xn, const u16* wr, float* r) {
    int row = blockIdx.x, l = threadIdx.x;
    float v[8]; float s = 0;
    for (int i = 0; i < 8; ++i) { v[i] = bf2f(x[row * D + l * 8 + i]); s += v[i]; }
    for (int m = 32; m >= 1; m >>= 1) s += __shfl_xor(s, m);
    float mean = s / D; float q = 0;
    for (int i = 0; i < 8; ++i) { v[i] -= mean; q += v[i] * v[i]; }
    for (int m = 32; m >= 1; m >>= 1) q += __shfl_xor(q, m);
    float rs = 1.0f / sqrtf(q / D + 1e-5f);
    float d = 0;
    for (int i = 0; i < 8; ++i) { u16 o = f2bf(v[i] * rs); xn[row * D + l * 8 + i] = o; if (wr) d += bf2f(o) * bf2f(wr[l * 8 + i]); }
    if (wr) { for (int m = 32; m >= 1; m >>= 1) d += __shfl_xor(d, m); if (l == 0) r[row] = d; }
}
__global__ void k_gemm(const u16* A, const u16* B, const u16* res, u16* C, int M, int N, int K) {
    __shared__ float sa[16][17], sb[16][17];
    int tx = threadIdx.x, ty = threadIdx.y; int row = blockIdx.y * 16 + ty, col = blockIdx.x * 16 + tx;
    float acc = 0;
    for (int k0 = 0; k0 < K; k0 += 16) {
        sa[ty][tx] = bf2f(A[row * K + k0 + tx]); sb[ty][tx] = bf2f(B[(k0 + ty) * N + col]);
        __syncthreads();
        for (int k = 0; k < 16; ++k) acc += sa[ty][k] * sb[k][tx];
        __syncthreads();
    }
    if (res) acc = bf2f(f2bf(acc)) + bf2f(res[row * N + col]);
    C[row * N + col] = f2bf(acc);
}
__global__ void k_attn(const u16* qkv, u16* o) {
    int h = blockIdx.x, q = blockIdx.y * 64 + threadIdx.x;
    float qv[DH], acc[DH]; for (int d = 0; d < DH; ++d) { qv[d] = bf2f(qkv[q * 1536 + h * 16 + d]); acc[d] = 0; }
    float m = -1e30f, l = 0;
    __shared__ float ks[64][16], vs[64][16];
    for (int j0 = 0; j0 < S; j0 += 64) {
        for (int d = 0; d < 16; ++d) { ks[threadIdx.x][d] = bf2f(qkv[(j0 + threadIdx.x) * 1536 + 512 + h * 16 + d]); vs[threadIdx.x][d] = bf2f(qkv[(j0 + threadIdx.x) * 1536 + 1024 + h * 16 + d]); }
        __syncthreads();
        for (int j = 0; j < 64; ++j) {
            float s = 0; for (int d = 0; d < 16; ++d) s += qv[d] * ks[j][d]; s *= 0.25f;
            float mn = fmaxf(m, s); float a = __expf(m - mn), p = __expf(s - mn);
            l = l * a + p; for (int d = 0; d < 16; ++d) acc[d] = acc[d] * a + p * vs[j][d]; m = mn;
        }
        __syncthreads();
    }
    for (int d = 0; d < 16; ++d) o[q * D + h * 16 + d] = f2bf(acc[d] / l);
}
__global__ void k_argmax(const float* r, int* sel) {
    __shared__ float bv[256]; __shared__ int bi[256];
    float b = -1e30f; int bidx = 0;
    for (int i = threadIdx.x; i < S; i += 256) { float v = bf2f(f2bf(r[i])); if (v > b) { b = v; bidx = i; } }
    bv[threadIdx.x] = b; bi[threadIdx.x] = bidx; __syncthreads();
    if (threadIdx.x == 0) { for (int i = 1; i < 256; ++i) if (bv[i] > b || (bv[i] == b && bi[i] < bidx)) { b = bv[i]; bidx = bi[i]; } sel[0] = bidx; }
}
__global__ void k_gateup(const u16* xn, const int* sel, const u16* wg, const u16* wu, float* hbuf) {
    int f = blockIdx.x * 64 + threadIdx.x; int s = sel[0]; float g = 0, u = 0;
    for (int k = 0; k < D; ++k) { float x = bf2f(xn[s * D + k]); g += x * bf2f(wg[k * F + f]); u += x * bf2f(wu[k * F + f]); }
    g = bf2f(f2bf(g)); u = bf2f(f2bf(u)); float sg = bf2f(f2bf(g / (1.0f + __expf(-g)))); hbuf[f] = bf2f(f2bf(sg * u));
}
__global__ void k_down(const float* hbuf, const u16* wd, const float* r, const int* sel, float* y) {
    int c = blockIdx.x * 64 + threadIdx.x; float acc = 0;
    for (int f = 0; f < F; ++f) acc += hbuf[f] * bf2f(wd[f * D + c]);
    float rv = bf2f(f2bf(r[sel[0]])); float sg = bf2f(f2bf(1.0f / (1.0f + __expf(-rv))));
    y[c] = bf2f(f2bf(bf2f(f2bf(acc)) * sg));
}
__global__ void k_bout(const u16* x, const u16* xn, const float* y, const int* sel, u16* out) {
    int row = blockIdx.x; int s = sel[0];
    for (int c = threadIdx.x; c < D; c += 64) { float t = bf2f(xn[row * D + c]); if (row == s) t = bf2f(f2bf(t + y[c])); out[row * D + c] = f2bf(bf2f(x[row * D + c]) + t); }
}
extern "C" void kernel_launch(void* const* d_in, const int* in_sizes, int n_in, void* d_out, int out_size, void* d_ws, size_t ws_size, hipStream_t stream) {
    const u16* x0 = (const u16*)d_in[0]; const u16* wqkv = (const u16*)d_in[1]; const u16* wo = (const u16*)d_in[2];
    const u16* wr = (const u16*)d_in[4]; const u16* wg = (const u16*)d_in[5]; const u16* wu = (const u16*)d_in[6]; const u16* wd = (const u16*)d_in[7];
    char* ws = (char*)d_ws;
    u16* xn = (u16*)ws; u16* qkv = (u16*)(ws + (2 << 20)); u16* o = (u16*)(ws + (8 << 20)); u16* x1 = (u16*)(ws + (10 << 20)); u16* x2 = (u16*)(ws + (12 << 20));
    float* r = (float*)(ws + (14 << 20)); int* sel = (int*)(ws + (15 << 20)); float* hb = (float*)(ws + (16 << 20)); float* y = (float*)(ws + (17 << 20));
    const u16* xin = x0;
    for (int layer = 0; layer < 2; ++layer) {
        u16* xout = layer == 0 ? x2 : (u16*)d_out;
        k_ln<<<S, 64, 0, stream>>>(xin, xn, nullptr, nullptr);
        k_gemm<<<dim3(1536 / 16, S / 16), dim3(16, 16), 0, stream>>>(xn, wqkv, nullptr, qkv, S, 1536, D);
        k_attn<<<dim3(H, S / 64), 64, 0, stream>>>(qkv, o);
        k_gemm<<<dim3(D / 16, S / 16), dim3(16, 16), 0, stream>>>(o, wo, xin, x1, S, D, D);
        k_ln<<<S, 64, 0, stream>>>(x1, xn, wr, r);
        k_argmax<<<1, 256, 0, stream>>>(r, sel);
        k_gateup<<<F / 64, 64, 0, stream>>>(xn, sel, wg, wu, hb);
        k_down<<<D / 64, 64, 0, stream>>>(hb, wd, r, sel, y);
        k_bout<<<S, 64, 0, stream>>>(x1, xn, y, sel, xout);
        xin = x2;
    }
}
```

```cpp
#include <hip/hip_runtime.h>
#include <stdint.h>

typedef unsigned short u16;
typedef unsigned u32;
typedef unsigned long long u64;
typedef float f32x16 __attribute__((ext_vector_type(16)));
typedef float f32x4 __attribute__((ext_vector_type(4)));
typedef float f32x2 __attribute__((ext_vector_type(2)));
typedef short bf16x8 __attribute__((ext_vector_type(8)));
typedef short s16x4 __attribute__((ext_vector_type(4)));
typedef unsigned u32x4 __attribute__((ext_vector_type(4)));
typedef unsigned u32x2 __attribute__((ext_vector_type(2)));
typedef __bf16 bf16x2 __attribute__((ext_vector_type(2)));
#define LAS __attribute__((address_space(3)))
#define GAS __attribute__((address_space(1)))
#define DI __device__ __forceinline__
#define MFMA(a, b, c) __builtin_amdgcn_mfma_f32_32x32x16_bf16((a), (b), (c), 0, 0, 0)
#define MFMA16(a, b, c) __builtin_amdgcn_mfma_f32_16x16x32_bf16((a), (b), (c), 0, 0, 0)

#define ATTN_LOOP_ASM \
    "ds_read_b128 v[104:107], %2\n\t" \
    "ds_read_b128 v[108:111], %3\n\t" \
    "ds_read_b128 v[112:115], %4\n\t" \
    "v_mov_b32 v100, 0\n\t" \
    "v_mov_b32 v101, 0\n\t" \
    "v_mov_b32 v102, 0\n\t" \
    "v_mov_b32 v103, 0\n\t" \
    "v_mov_b32 v124, 0\n\t" \
    "v_mov_b32 v125, 0\n\t" \
    "v_mov_b32 v126, 0\n\t" \
    "v_mov_b32 v127, 0\n\t" \
    "s_mov_b32 %8, 0\n\t" \
    "s_waitcnt lgkmcnt(0)\n\t" \
    "v_mfma_f32_32x32x16_bf16 v[64:79], v[104:107], %9, 0\n\t" \
    "s_nop 7\n\t" \
    "s_nop 3\n\t" \
    "1:\n\t" \
    "v_mfma_f32_32x32x16_bf16 v[80:95], v[104:107], %10, 0\n\t" \
    "ds_read_b128 v[116:119], %2 offset:2128\n\t" \
    "ds_read_b128 v[120:123], %3 offset:2128\n\t" \
    "ds_read_b32 v128, %6\n\t" \
    "v_exp_f32 v64, v64\n\t" \
    "v_exp_f32 v65, v65\n\t" \
    "v_exp_f32 v66, v66\n\t" \
    "v_exp_f32 v67, v67\n\t" \
    "v_exp_f32 v68, v68\n\t" \
    "v_exp_f32 v69, v69\n\t" \
    "v_mfma_f32_32x32x16_bf16 %1, v[124:127], v[100:103], %1\n\t" \
    "ds_read_b128 v[124:127], %4 offset:2128\n\t" \
    "v_exp_f32 v70, v70\n\t" \
    "v_exp_f32 v71, v71\n\t" \
    "v_exp_f32 v72, v72\n\t" \
    "v_cvt_pk_bf16_f32 v96, v64, v65\n\t" \
    "v_cvt_pk_bf16_f32 v97, v66, v67\n\t" \
    "v_cvt_pk_bf16_f32 v98, v68, v69\n\t" \
    "v_cvt_pk_bf16_f32 v99, v70, v71\n\t" \
    "v_exp_f32 v73, v73\n\t" \
    "v_exp_f32 v74, v74\n\t" \
    "v_mfma_f32_32x32x16_bf16 %0, v[108:111], v[96:99], %0\n\t" \
    "v_exp_f32 v75, v75\n\t" \
    "v_exp_f32 v76, v76\n\t" \
    "v_exp_f32 v77, v77\n\t" \
    "v_exp_f32 v78, v78\n\t" \
    "v_exp_f32 v79, v79\n\t" \
    "v_cvt_pk_bf16_f32 v100, v72, v73\n\t" \
    "v_cvt_pk_bf16_f32 v101, v74, v75\n\t" \
    "v_cvt_pk_bf16_f32 v102, v76, v77\n\t" \
    "v_cvt_pk_bf16_f32 v103, v78, v79\n\t" \
    "s_waitcnt lgkmcnt(0)\n\t" \
    "v_mfma_f32_32x32x16_bf16 v[64:79], v[116:119], %9, 0\n\t" \
    "v_exp_f32 v80, v80\n\t" \
    "v_exp_f32 v81, v81\n\t" \
    "v_exp_f32 v82, v82\n\t" \
    "v_exp_f32 v83, v83\n\t" \
    "v_exp_f32 v84, v84\n\t" \
    "v_exp_f32 v85, v85\n\t" \
    "v_mfma_f32_32x32x16_bf16 %0, v[112:115], v[100:103], %0\n\t" \
    "v_exp_f32 v86, v86\n\t" \
    "v_exp_f32 v87, v87\n\t" \
    "v_exp_f32 v88, v88\n\t" \
    "v_cvt_pk_bf16_f32 v96, v80, v81\n\t" \
    "v_cvt_pk_bf16_f32 v97, v82, v83\n\t" \
    "v_cvt_pk_bf16_f32 v98, v84, v85\n\t" \
    "v_cvt_pk_bf16_f32 v99, v86, v87\n\t" \
    "v_exp_f32 v89, v89\n\t" \
    "v_exp_f32 v90, v90\n\t" \
    "v_mfma_f32_32x32x16_bf16 %1, v[108:111], v[96:99], %1\n\t" \
    "v_exp_f32 v91, v91\n\t" \
    "v_exp_f32 v92, v92\n\t" \
    "v_exp_f32 v93, v93\n\t" \
    "v_exp_f32 v94, v94\n\t" \
    "v_exp_f32 v95, v95\n\t" \
    "v_cvt_pk_bf16_f32 v100, v88, v89\n\t" \
    "v_cvt_pk_bf16_f32 v101, v90, v91\n\t" \
    "v_cvt_pk_bf16_f32 v102, v92, v93\n\t" \
    "v_cvt_pk_bf16_f32 v103, v94, v95\n\t" \
    "v_mfma_f32_32x32x16_bf16 v[80:95], v[116:119], %10, 0\n\t" \
    "ds_read_b128 v[104:107], %2 offset:4256\n\t" \
    "ds_read_b128 v[108:111], %3 offset:4256\n\t" \
    "v_exp_f32 v64, v64\n\t" \
    "v_exp_f32 v65, v65\n\t" \
    "v_exp_f32 v66, v66\n\t" \
    "v_exp_f32 v67, v67\n\t" \
    "v_exp_f32 v68, v68\n\t" \
    "v_exp_f32 v69, v69\n\t" \
    "v_mfma_f32_32x32x16_bf16 %1, v[112:115], v[100:103], %1\n\t" \
    "ds_read_b128 v[112:115], %4 offset:4256\n\t" \
    "v_exp_f32 v70, v70\n\t" \
    "v_exp_f32 v71, v71\n\t" \
    "v_exp_f32 v72, v72\n\t" \
    "v_cvt_pk_bf16_f32 v96, v64, v65\n\t" \
    "v_cvt_pk_bf16_f32 v97, v66, v67\n\t" \
    "v_cvt_pk_bf16_f32 v98, v68, v69\n\t" \
    "v_cvt_pk_bf16_f32 v99, v70, v71\n\t" \
    "v_exp_f32 v73, v73\n\t" \
    "v_exp_f32 v74, v74\n\t" \
    "v_mfma_f32_32x32x16_bf16 %0, v[120:123], v[96:99], %0\n\t" \
    "v_exp_f32 v75, v75\n\t" \
    "v_exp_f32 v76, v76\n\t" \
    "v_exp_f32 v77, v77\n\t" \
    "v_exp_f32 v78, v78\n\t" \
    "v_exp_f32 v79, v79\n\t" \
    "v_cvt_pk_bf16_f32 v100, v72, v73\n\t" \
    "v_cvt_pk_bf16_f32 v101, v74, v75\n\t" \
    "v_cvt_pk_bf16_f32 v102, v76, v77\n\t" \
    "v_cvt_pk_bf16_f32 v103, v78, v79\n\t" \
    "s_waitcnt lgkmcnt(0)\n\t" \
    "v_mfma_f32_32x32x16_bf16 v[64:79], v[104:107], %9, 0\n\t" \
    "v_exp_f32 v80, v80\n\t" \
    "v_exp_f32 v81, v81\n\t" \
    "v_exp_f32 v82, v82\n\t" \
    "v_exp_f32 v83, v83\n\t" \
    "v_exp_f32 v84, v84\n\t" \
    "v_exp_f32 v85, v85\n\t" \
    "v_mfma_f32_32x32x16_bf16 %0, v[124:127], v[100:103], %0\n\t" \
    "v_exp_f32 v86, v86\n\t" \
    "v_exp_f32 v87, v87\n\t" \
    "v_exp_f32 v88, v88\n\t" \
    "v_cvt_pk_bf16_f32 v96, v80, v81\n\t" \
    "v_cvt_pk_bf16_f32 v97, v82, v83\n\t" \
    "v_cvt_pk_bf16_f32 v98, v84, v85\n\t" \
    "v_cvt_pk_bf16_f32 v99, v86, v87\n\t" \
    "v_exp_f32 v89, v89\n\t" \
    "v_exp_f32 v90, v90\n\t" \
    "v_mfma_f32_32x32x16_bf16 %1, v[120:123], v[96:99], %1\n\t" \
    "v_exp_f32 v91, v91\n\t" \
    "v_exp_f32 v92, v92\n\t" \
    "v_exp_f32 v93, v93\n\t" \
    "v_exp_f32 v94, v94\n\t" \
    "v_exp_f32 v95, v95\n\t" \
    "v_cvt_pk_bf16_f32 v100, v88, v89\n\t" \
    "v_cvt_pk_bf16_f32 v101, v90, v91\n\t" \
    "v_cvt_pk_bf16_f32 v102, v92, v93\n\t" \
    "v_cvt_pk_bf16_f32 v103, v94, v95\n\t" \
    "v_mfma_f32_32x32x16_bf16 v[80:95], v[104:107], %10, 0\n\t" \
    "ds_read_b128 v[116:119], %2 offset:6384\n\t" \
    "ds_read_b128 v[120:123], %3 offset:6384\n\t" \
    "v_exp_f32 v64, v64\n\t" \
    "v_exp_f32 v65, v65\n\t" \
    "v_exp_f32 v66, v66\n\t" \
    "v_exp_f32 v67, v67\n\t" \
    "v_exp_f32 v68, v68\n\t" \
    "v_exp_f32 v69, v69\n\t" \
    "v_mfma_f32_32x32x16_bf16 %1, v[124:127], v[100:103], %1\n\t" \
    "ds_read_b128 v[124:127], %4 offset:6384\n\t" \
    "v_exp_f32 v70, v70\n\t" \
    "v_exp_f32 v71, v71\n\t" \
    "v_exp_f32 v72, v72\n\t" \
    "v_cvt_pk_bf16_f32 v96, v64, v65\n\t" \
    "v_cvt_pk_bf16_f32 v97, v66, v67\n\t" \
    "v_cvt_pk_bf16_f32 v98, v68, v69\n\t" \
    "v_cvt_pk_bf16_f32 v99, v70, v71\n\t" \
    "v_exp_f32 v73, v73\n\t" \
    "v_exp_f32 v74, v74\n\t" \
    "v_mfma_f32_32x32x16_bf16 %0, v[108:111], v[96:99], %0\n\t" \
    "v_exp_f32 v75, v75\n\t" \
    "v_exp_f32 v76, v76\n\t" \
    "v_exp_f32 v77, v77\n\t" \
    "v_exp_f32 v78, v78\n\t" \
    "v_exp_f32 v79, v79\n\t" \
    "v_cvt_pk_bf16_f32 v100, v72, v73\n\t" \
    "v_cvt_pk_bf16_f32 v101, v74, v75\n\t" \
    "v_cvt_pk_bf16_f32 v102, v76, v77\n\t" \
    "v_cvt_pk_bf16_f32 v103, v78, v79\n\t" \
    "s_waitcnt lgkmcnt(0)\n\t" \
    "v_mfma_f32_32x32x16_bf16 v[64:79], v[116:119], %9, 0\n\t" \
    "v_exp_f32 v80, v80\n\t" \
    "v_exp_f32 v81, v81\n\t" \
    "v_exp_f32 v82, v82\n\t" \
    "v_exp_f32 v83, v83\n\t" \
    "v_exp_f32 v84, v84\n\t" \
    "v_exp_f32 v85, v85\n\t" \
    "v_mfma_f32_32x32x16_bf16 %0, v[112:115], v[100:103], %0\n\t" \
    "v_exp_f32 v86, v86\n\t" \
    "v_exp_f32 v87, v87\n\t" \
    "v_exp_f32 v88, v88\n\t" \
    "v_cvt_pk_bf16_f32 v96, v80, v81\n\t" \
    "v_cvt_pk_bf16_f32 v97, v82, v83\n\t" \
    "v_cvt_pk_bf16_f32 v98, v84, v85\n\t" \
    "v_cvt_pk_bf16_f32 v99, v86, v87\n\t" \
    "v_exp_f32 v89, v89\n\t" \
    "v_exp_f32 v90, v90\n\t" \
    "v_mfma_f32_32x32x16_bf16 %1, v[108:111], v[96:99], %1\n\t" \
    "v_exp_f32 v91, v91\n\t" \
    "v_exp_f32 v92, v92\n\t" \
    "v_exp_f32 v93, v93\n\t" \
    "v_exp_f32 v94, v94\n\t" \
    "v_exp_f32 v95, v95\n\t" \
    "v_cvt_pk_bf16_f32 v100, v88, v89\n\t" \
    "v_cvt_pk_bf16_f32 v101, v90, v91\n\t" \
    "v_cvt_pk_bf16_f32 v102, v92, v93\n\t" \
    "v_cvt_pk_bf16_f32 v103, v94, v95\n\t" \
    "v_readfirstlane_b32 %7, v128\n\t" \
    "s_nop 0\n\t" \
    "s_cmp_lg_u32 %7, 0\n\t" \
    "s_cbranch_scc1 4f\n\t" \
    "3:\n\t" \
    "s_sleep 1\n\t" \
    "ds_read_b32 v128, %6\n\t" \
    "s_waitcnt lgkmcnt(0)\n\t" \
    "v_readfirstlane_b32 %7, v128\n\t" \
    "s_add_u32 %8, %8, 1\n\t" \
    "s_cmp_gt_u32 %8, %12\n\t" \
    "s_cbranch_scc1 4f\n\t" \
    "s_cmp_eq_u32 %7, 0\n\t" \
    "s_cbranch_scc1 3b\n\t" \
    "4:\n\t" \
    "v_mfma_f32_32x32x16_bf16 v[80:95], v[116:119], %10, 0\n\t" \
    "ds_read_b128 v[104:107], %2 offset:8512\n\t" \
    "ds_read_b128 v[108:111], %3 offset:8512\n\t" \
    "v_exp_f32 v64, v64\n\t" \
    "v_exp_f32 v65, v65\n\t" \
    "v_exp_f32 v66, v66\n\t" \
    "v_exp_f32 v67, v67\n\t" \
    "v_exp_f32 v68, v68\n\t" \
    "v_exp_f32 v69, v69\n\t" \
    "v_mfma_f32_32x32x16_bf16 %1, v[112:115], v[100:103], %1\n\t" \
    "ds_read_b128 v[112:115], %4 offset:8512\n\t" \
    "v_exp_f32 v70, v70\n\t" \
    "v_exp_f32 v71, v71\n\t" \
    "v_exp_f32 v72, v72\n\t" \
    "v_cvt_pk_bf16_f32 v96, v64, v65\n\t" \
    "v_cvt_pk_bf16_f32 v97, v66, v67\n\t" \
    "v_cvt_pk_bf16_f32 v98, v68, v69\n\t" \
    "v_cvt_pk_bf16_f32 v99, v70, v71\n\t" \
    "v_exp_f32 v73, v73\n\t" \
    "v_exp_f32 v74, v74\n\t" \
    "v_mfma_f32_32x32x16_bf16 %0, v[120:123], v[96:99], %0\n\t" \
    "v_exp_f32 v75, v75\n\t" \
    "v_exp_f32 v76, v76\n\t" \
    "v_exp_f32 v77, v77\n\t" \
    "v_exp_f32 v78, v78\n\t" \
    "v_exp_f32 v79, v79\n\t" \
    "v_cvt_pk_bf16_f32 v100, v72, v73\n\t" \
    "v_cvt_pk_bf16_f32 v101, v74, v75\n\t" \
    "v_cvt_pk_bf16_f32 v102, v76, v77\n\t" \
    "v_cvt_pk_bf16_f32 v103, v78, v79\n\t" \
    "s_waitcnt lgkmcnt(0)\n\t" \
    "v_mfma_f32_32x32x16_bf16 v[64:79], v[104:107], %9, 0\n\t" \
    "v_exp_f32 v80, v80\n\t" \
    "v_exp_f32 v81, v81\n\t" \
    "v_exp_f32 v82, v82\n\t" \
    "v_exp_f32 v83, v83\n\t" \
    "v_exp_f32 v84, v84\n\t" \
    "v_exp_f32 v85, v85\n\t" \
    "v_mfma_f32_32x32x16_bf16 %0, v[124:127], v[100:103], %0\n\t" \
    "v_exp_f32 v86, v86\n\t" \
    "v_exp_f32 v87, v87\n\t" \
    "v_exp_f32 v88, v88\n\t" \
    "v_cvt_pk_bf16_f32 v96, v80, v81\n\t" \
    "v_cvt_pk_bf16_f32 v97, v82, v83\n\t" \
    "v_cvt_pk_bf16_f32 v98, v84, v85\n\t" \
    "v_cvt_pk_bf16_f32 v99, v86, v87\n\t" \
    "v_exp_f32 v89, v89\n\t" \
    "v_exp_f32 v90, v90\n\t" \
    "v_mfma_f32_32x32x16_bf16 %1, v[120:123], v[96:99], %1\n\t" \
    "v_exp_f32 v91, v91\n\t" \
    "v_exp_f32 v92, v92\n\t" \
    "v_exp_f32 v93, v93\n\t" \
    "v_exp_f32 v94, v94\n\t" \
    "v_exp_f32 v95, v95\n\t" \
    "v_cvt_pk_bf16_f32 v100, v88, v89\n\t" \
    "v_cvt_pk_bf16_f32 v101, v90, v91\n\t" \
    "v_cvt_pk_bf16_f32 v102, v92, v93\n\t" \
    "v_cvt_pk_bf16_f32 v103, v94, v95\n\t" \
    "v_add_u32 %2, 8512, %2\n\t" \
    "v_add_u32 %3, 8512, %3\n\t" \
    "v_add_u32 %4, 8512, %4\n\t" \
    "v_add_u32 %6, 4, %6\n\t" \
    "s_sub_u32 %5, %5, 4\n\t" \
    "s_cmp_gt_u32 %5, %11\n\t" \
    "s_cbranch_scc1 2f\n\t" \
    "s_setprio 0\n\t" \
    "2:\n\t" \
    "s_cmp_lg_u32 %7, 1\n\t" \
    "s_cbranch_scc1 5f\n\t" \
    "s_cmp_lg_u32 %5, 0\n\t" \
    "s_cbranch_scc1 1b\n\t" \
    "5:\n\t" \
    "s_nop 1\n\t" \
    "v_mfma_f32_32x32x16_bf16 %1, v[124:127], v[100:103], %1\n\t" \
    "s_waitcnt lgkmcnt(0)\n\t" \
    "s_nop 7\n\t" \
    "s_nop 7\n\t" \
    ""
#define ATTN_LOOP_CLOBBERS "v64", "v65", "v66", "v67", "v68", "v69", "v70", "v71", "v72", "v73", "v74", "v75", "v76", "v77", "v78", "v79", "v80", "v81", "v82", "v83", "v84", "v85", "v86", "v87", "v88", "v89", "v90", "v91", "v92", "v93", "v94", "v95", "v96", "v97", "v98", "v99", "v100", "v101", "v102", "v103", "v104", "v105", "v106", "v107", "v108", "v109", "v110", "v111", "v112", "v113", "v114", "v115", "v116", "v117", "v118", "v119", "v120", "v121", "v122", "v123", "v124", "v125", "v126", "v127", "v128"

constexpr int S_ = 2048, D_ = 512, F_ = 1792;
constexpr float EPS = 1e-5f;
constexpr float QSCALE = 0.25f * 1.4426950408889634f;
constexpr float BOUND2 = 4096.f;

constexpr u32 WS_EPOCH = 0;
constexpr u32 WS_FKV = 4096;
constexpr u32 WS_FO = WS_FKV + 2 * 256 * 128;
constexpr u32 WS_FX = WS_FO + 2 * 256 * 128;
constexpr u32 WS_CAND = WS_FX + 2 * 256 * 128;
constexpr u32 WS_ST0 = WS_CAND + 2 * 256 * 128;
constexpr u32 WS_ST1 = WS_ST0 + 256 * 128;
constexpr u32 WS_HG = WS_ST1 + 2048 * 16;
constexpr u32 WS_X2S = WS_HG + 2 * 1792 * 8;
constexpr u32 WS_QKVS = WS_X2S + 64 * 128;
constexpr u32 WS_RQ = WS_QKVS + 1536 * 8;
constexpr u32 WS_SYNC_END = WS_RQ + 256 * 128;
constexpr u32 WS_PST = 1u << 20;
constexpr u32 WS_KV = 2u << 20;
constexpr u32 WS_O = 10u << 20;
constexpr u32 WS_XF = 14u << 20;
constexpr u32 WS_DBG = 24u << 20;
constexpr u32 WS_QDBG = 28u << 20;

constexpr u32 RS = 2128;
constexpr u32 L_MINI = 64 * RS;
constexpr u32 L_FFN = 142080;
constexpr u32 L_P0W = 0;
constexpr u32 L_P0X = 49152;
constexpr u32 L_MX = 0;
constexpr u32 L_W1 = 8512;
constexpr u32 L_SCR = 57664;
constexpr u32 L_WO = 76608;
constexpr u32 WO1 = 32768 + 64;
constexpr u32 L_PERS = 147456;
constexpr u32 L_C = L_PERS;
constexpr u32 L_CM = L_C + 1024;
constexpr u32 L_RA = L_CM + 1024;
constexpr u32 L_RB = L_RA + 1024;
constexpr u32 L_CS = L_RB + 1024;
constexpr u32 L_QX = L_CS + 256;
constexpr u32 L_MISC = L_QX + 8192;
constexpr u32 L_PS = L_MISC + 512;
constexpr u32 L_FIN = L_PS + 2048;
constexpr u32 F_XN = L_FFN;
constexpr u32 F_RED = L_FFN + 4096;
constexpr u32 F_X2 = L_FFN + 4608;
constexpr u32 F_QS = L_FFN + 4864;
constexpr u32 LDS_BYTES = 163840;
static_assert(L_FIN + 512 <= LDS_BYTES && L_WO + WO1 + 32768 <= L_FFN + 256 && F_QS + 32 <= L_PERS && L_MINI + RS <= L_FFN && L_SCR + 18432 <= 32 * RS + 8512 && L_W1 + 49152 <= L_SCR, "lds");
constexpr u32 M_KM = 0;
constexpr u32 M_QM = 8;
constexpr u32 M_KALL = 16;
constexpr u32 M_RDY = 20;
constexpr u32 M_SEL = 21;
constexpr u32 M_CNT = 22;
constexpr u32 M_MINI = 23;
constexpr u32 M_SELV = 24;
constexpr u32 M_KNEW = 28;
constexpr u32 M_SEL2 = 27;
constexpr u32 M_PS = 30;
constexpr u32 M_FIN = 31;
constexpr u32 M_KC = 43;
constexpr u32 M_WP = 44;
constexpr u32 M_RD2 = 45;
constexpr u32 M_QR = 46;
constexpr u32 M_FLG = 64;
constexpr u32 M_NRD = 16;
constexpr u32 M_OF = 32;
constexpr u32 M_XF = 33;
constexpr u32 M_OC = 37;
constexpr u32 M_XC = 38;
constexpr u32 M_RV = 39;
constexpr u32 M_XN = 42;
constexpr u32 M_STAT = 29;

constexpr u32 SPIN_CAP = 1u << 14;
#define PF3 13
#define CODE_LINES 0

DI unsigned cvtpk(float lo, float hi) { f32x2 v = {lo, hi}; bf16x2 b = __builtin_convertvector(v, bf16x2); return __builtin_bit_cast(unsigned, b); }
DI float bflo(u32 p) { return __uint_as_float(p << 16); }
DI float bfhi(u32 p) { return __uint_as_float(p & 0xffff0000u); }
DI float rbf(float x) { return bflo(cvtpk(x, x) & 0xffffu); }
DI float dot2(u32 a, u32 b, float c) { return __builtin_amdgcn_fdot2_f32_bf16(__builtin_bit_cast(bf16x2, a), __builtin_bit_cast(bf16x2, b), c, false); }
DI float swapadd(float x) { auto r = __builtin_amdgcn_permlane32_swap(__float_as_uint(x), __float_as_uint(x), false, false); return __uint_as_float(r[0]) + __uint_as_float(r[1]); }
DI float swapmax(float x) { auto r = __builtin_amdgcn_permlane32_swap(__float_as_uint(x), __float_as_uint(x), false, false); return fmaxf(__uint_as_float(r[0]), __uint_as_float(r[1])); }
template <int CTRL> DI float dppf(float x) { return __int_as_float(__builtin_amdgcn_update_dpp(0, __float_as_int(x), CTRL, 0xf, 0xf, true)); }
DI float swap16add(float x) { auto r = __builtin_amdgcn_permlane16_swap(__float_as_uint(x), __float_as_uint(x), false, false); return __uint_as_float(r[0]) + __uint_as_float(r[1]); }
DI float swap16max(float x) { auto r = __builtin_amdgcn_permlane16_swap(__float_as_uint(x), __float_as_uint(x), false, false); return fmaxf(__uint_as_float(r[0]), __uint_as_float(r[1])); }
DI float wave_max(float x) { x = fmaxf(x, dppf<0x128>(x)); x = fmaxf(x, dppf<0x124>(x)); x = fmaxf(x, dppf<0x122>(x)); x = fmaxf(x, dppf<0x121>(x)); return swapmax(swap16max(x)); }
DI float wave_sum(float x) { x += dppf<0x128>(x); x += dppf<0x124>(x); x += dppf<0x122>(x); x += dppf<0x121>(x); return swapadd(swap16add(x)); }
DI float tr32(float a, float b) { auto r = __builtin_amdgcn_permlane32_swap(__float_as_uint(a), __float_as_uint(b), false, false); return __uint_as_float(r[0]) + __uint_as_float(r[1]); }
DI float tr16(float a, float b) { auto r = __builtin_amdgcn_permlane16_swap(__float_as_uint(a), __float_as_uint(b), false, false); return __uint_as_float(r[0]) + __uint_as_float(r[1]); }
DI int fresh_lane() { int l; asm volatile("v_mbcnt_lo_u32_b32 %0, -1, 0\n\tv_mbcnt_hi_u32_b32 %0, -1, %0" : "=v"(l)); return l; }
DI u32 rfl(u32 x) { return __builtin_amdgcn_readfirstlane(x); }
DI void bar() { asm volatile("s_waitcnt lgkmcnt(0)\n\ts_barrier" ::: "memory"); }
#define PACE_N 4
#define PACE() do { asm volatile("" ::: "memory"); if (PACE_N) __builtin_amdgcn_s_sleep(PACE_N); } while (0)
#define MEMPIN() asm volatile("" ::: "memory")
DI void vm0() { asm volatile("s_waitcnt vmcnt(0)" ::: "memory"); }

template <class T> DI T ldl(u32 a) { return *(const LAS T*)(size_t)a; }
template <class T> DI void stl(u32 a, T v) { *(LAS T*)(size_t)a = v; }
DI s16x4 trd(u32 a) { typedef short v4 __attribute__((ext_vector_type(4))); return __builtin_bit_cast(s16x4, __builtin_amdgcn_ds_read_tr16_b64_v4i16((LAS v4*)(size_t)a)); }
DI bf16x8 trfrag(u32 a, u32 second) { s16x4 lo = trd(a), hi = trd(a + second); return __builtin_shufflevector(lo, hi, 0, 1, 2, 3, 4, 5, 6, 7); }

DI void glds(u32 voff, const void* sbase, u32 dst) {
    u32 keep;
    asm volatile("s_mov_b32 %0, m0\n\ts_mov_b32 m0, %3\n\ts_nop 0\n\tglobal_load_lds_dwordx4 %1, %2\n\ts_mov_b32 m0, %0" : "=&s"(keep) : "v"(voff), "s"(sbase), "s"(dst) : "memory");
}
DI void glds_sc1(u32 voff, const void* sbase, u32 dst) {
    u32 keep;
    asm volatile("s_mov_b32 %0, m0\n\ts_mov_b32 m0, %3\n\ts_nop 0\n\tglobal_load_lds_dwordx4 %1, %2 sc1\n\ts_mov_b32 m0, %0" : "=&s"(keep) : "v"(voff), "s"(sbase), "s"(dst) : "memory");
}

DI void glds4(u32 voff, const void* sbase, u32 dst) {
    u32 keep;
    asm volatile("s_mov_b32 %0, m0\n\ts_mov_b32 m0, %3\n\ts_nop 0\n\tglobal_load_lds_dword %1, %2 sc1\n\ts_mov_b32 m0, %0" : "=&s"(keep) : "v"(voff), "s"(sbase), "s"(dst) : "memory");
}
typedef __amdgpu_buffer_rsrc_t rsrc_t;
DI u32x4 ld16(rsrc_t r, u32 off) { return __builtin_amdgcn_raw_buffer_load_b128(r, off, 0, 16); }
DI void st16(rsrc_t r, u32 off, u32x4 v) { __builtin_amdgcn_raw_buffer_store_b128(v, r, off, 0, 16); }
DI u32 ldf32(char* ws, u32 off) { return __hip_atomic_load((GAS u32*)(ws + off), __ATOMIC_RELAXED, __HIP_MEMORY_SCOPE_AGENT); }
DI u64 ldf64(char* ws, u32 off) { return __hip_atomic_load((GAS u64*)(ws + off), __ATOMIC_RELAXED, __HIP_MEMORY_SCOPE_AGENT); }
DI void stf32(char* ws, u32 off, u32 v) { __hip_atomic_store((GAS u32*)(ws + off), v, __ATOMIC_RELAXED, __HIP_MEMORY_SCOPE_AGENT); }
DI void stf64(char* ws, u32 off, u64 v) { __hip_atomic_store((GAS u64*)(ws + off), v, __ATOMIC_RELAXED, __HIP_MEMORY_SCOPE_AGENT); }
DI u64 gran(u32 ep, u32 v) { return ((u64)ep << 32) | v; }
DI u64 gran(u32 ep, float v) { return ((u64)ep << 32) | __float_as_uint(v); }

DI void wait_flags32(char* ws, u32 off, bool act, u32 ep) {
#pragma nounroll
    for (u32 sp = 0; sp < SPIN_CAP; ++sp) {
        u32 v = act ? ldf32(ws, off) : ep;
        if (__all(v == ep)) break;
        __builtin_amdgcn_s_sleep(1);
    }
}
DI u32 wait_gran(char* ws, u32 off, bool act, u32 ep) {
    u64 v = 0;
#pragma nounroll
    for (u32 sp = 0; sp < SPIN_CAP; ++sp) {
        v = act ? ldf64(ws, off) : gran(ep, 0u);
        if (__all((u32)(v >> 32) == ep)) break;
        __builtin_amdgcn_s_sleep(1);
    }
    return (u32)v;
}
template <int N, int STRIDE, int PAUSE> DI void sweep_grans(rsrc_t rs, u32 off, u32 ep, u32 (&out)[N]) {
#pragma nounroll
    for (u32 sp = 0; sp < SPIN_CAP; ++sp) {
        bool ok = true;
#pragma unroll
        for (int i = 0; i < N; ++i) { const u32x2 v = __builtin_amdgcn_raw_buffer_load_b64(rs, off + i * STRIDE, 0, 16); out[i] = v[0]; ok &= v[1] == ep; }
        if (__all(ok)) break;
        __builtin_amdgcn_s_sleep(PAUSE);
    }
}
template <int N> DI void wait_grans(char* ws, u32 off, u32 stride, u32 ep, u32 (&out)[N]) {
#pragma nounroll
    for (u32 sp = 0; sp < SPIN_CAP; ++sp) {
        bool ok = true;
#pragma unroll
        for (int i = 0; i < N; ++i) { u64 v = ldf64(ws, off + i * stride); out[i] = (u32)v; ok &= (u32)(v >> 32) == ep; }
        if (__all(ok)) break;
        __builtin_amdgcn_s_sleep(1);
    }
}
template <bool LAST> DI u32x4 wait_rec(rsrc_t rs, u32 off, u32 ep) {
    u32x4 v;
#pragma nounroll
    for (u32 sp = 0; sp < SPIN_CAP; ++sp) {
        v = ld16(rs, off);
        if (__all(LAST ? v[3] == ep : (v[1] == ep && v[3] == ep))) break;
        __builtin_amdgcn_s_sleep(1);
    }
    return v;
}
DI void wait_gran_slow(char* ws, u32 off, u32 ep) {
#pragma nounroll
    for (u32 sp = 0; sp < SPIN_CAP; ++sp) { if ((u32)(ldf64(ws, off) >> 32) == ep) break; __builtin_amdgcn_s_sleep(8); }
}
DI void wait_lds_slow(u32 a, u32 want) {
#pragma nounroll
    for (u32 sp = 0; sp < (SPIN_CAP << 2); ++sp) { if (rfl(*(volatile LAS u32*)(size_t)a) >= want) break; __builtin_amdgcn_s_sleep(8); }
    asm volatile("" ::: "memory");
}
DI bool arrive(u32 word, u32 total) {
    asm volatile("s_waitcnt lgkmcnt(0)" ::: "memory");
    u32 old = 0;
    if (fresh_lane() == 0) old = __hip_atomic_fetch_add((LAS u32*)(size_t)(L_MISC + word * 4), 1u, __ATOMIC_RELAXED, __HIP_MEMORY_SCOPE_WORKGROUP);
    old = rfl(old);
    asm volatile("" ::: "memory");
    return old + 1u == total;
}
DI void lds_inc(u32 word) {
    asm volatile("s_waitcnt lgkmcnt(0)" ::: "memory");
    if (fresh_lane() == 0) __hip_atomic_fetch_add((LAS u32*)(size_t)(L_MISC + word * 4), 1u, __ATOMIC_RELAXED, __HIP_MEMORY_SCOPE_WORKGROUP);
}
DI void wait_lds(u32 a, u32 want) {
#pragma nounroll
    for (u32 sp = 0; sp < (SPIN_CAP << 4); ++sp) {
        u32 v = *(volatile LAS u32*)(size_t)a;
        if (rfl(v) >= want) break;
        __builtin_amdgcn_s_sleep(1);
    }
    asm volatile("" ::: "memory");
}

#define STAMP(i) do {} while (0)
#define STAMP_DUMP() do {} while (0)
#define CSTAMP(i) do {} while (0)

struct Ctx {
    const u16 *x0, *wqkv, *wo, *wr, *wg, *wu, *wd; u16* out; char* ws;
    rsrc_t rs; int bid, w, qr, h; u32 ep;
    u32 epv; const char* pc;
};

template <bool SLOW>
DI void tile_one(u32 ka, u32 v0a, u32 v1a, bf16x8 q, f32x16& o, float& m) {
    bf16x8 kf = ldl<bf16x8>(ka), v0 = ldl<bf16x8>(v0a), v1 = ldl<bf16x8>(v1a);
    f32x16 z = {};
    f32x16 s = MFMA(kf, q, z);
    if (SLOW) {
        float t = s[0];
#pragma unroll
        for (int i = 1; i < 16; ++i) t = fmaxf(t, s[i]);
        t = swapmax(t);
        float mn = fmaxf(m, t);
        float al = __builtin_amdgcn_exp2f(m - mn);
#pragma unroll
        for (int i = 0; i < 16; ++i) { o[i] *= al; s[i] -= mn; }
        m = mn;
    }
    u32x4 p0, p1;
#pragma unroll
    for (int i = 0; i < 4; ++i) {
        p0[i] = cvtpk(__builtin_amdgcn_exp2f(s[2 * i]), __builtin_amdgcn_exp2f(s[2 * i + 1]));
        p1[i] = cvtpk(__builtin_amdgcn_exp2f(s[8 + 2 * i]), __builtin_amdgcn_exp2f(s[9 + 2 * i]));
    }
    o = MFMA(v0, __builtin_bit_cast(bf16x8, p0), o);
    o = MFMA(v1, __builtin_bit_cast(bf16x8, p1), o);
}

template <int L>
DI void qkv_phase(Ctx& c) {
    const int w = c.w, lane = fresh_lane(), t = lane & 15, g = lane >> 4;
    char* ws = c.ws;
    const int mt = c.qr * 8 + w;
    const u32 WB = L ? L_W1 : L_P0W;
    const u32 aw = WB + (16 * (g >> 1) + 4 * (g & 1) + (t >> 2)) * 32 + (t & 3) * 8;
    f32x4 aq[2] = {}, ak[2] = {}, av[2] = {};
    const u32 rot = c.h & 7;
    auto kstep = [&](u32 a0w, u32x4 x0, u32x4 x1) {
        const bf16x8 wq = trfrag(a0w, 256), wk = trfrag(a0w + 16384, 256), wv = trfrag(a0w + 32768, 256);
        const bf16x8 a0 = __builtin_bit_cast(bf16x8, x0), a1 = __builtin_bit_cast(bf16x8, x1);
        aq[0] = MFMA16(wq, a0, aq[0]); aq[1] = MFMA16(wq, a1, aq[1]);
        ak[0] = MFMA16(wk, a0, ak[0]); ak[1] = MFMA16(wk, a1, ak[1]);
        av[0] = MFMA16(a0, wv, av[0]); av[1] = MFMA16(a1, wv, av[1]);
    };
    if (L == 0) {
        const u32 wp = lane >> 1, wrow = (wp & ~12u) | ((wp & 4u) << 1) | ((wp & 8u) >> 1);
        const u32 wof = wrow * 3072 + ((c.h * 16 + (lane & 1) * 8) << 1);
        auto issue_w = [&](int half) {
#pragma unroll
            for (int i = 0; i < 3; ++i) { const int m = w * 3 + i, img = m >> 3; const u32 blk = (half * 8 + (m & 7) + 2 * rot) & 15; glds(wof, (const char*)c.wqkv + blk * 32 * 3072 + img * 1024, WB + img * 16384 + blk * 1024); }
        };
        u32 xoff[4], ard[4];
#pragma unroll
        for (int m = 0; m < 4; ++m) { u32 row = 8 * m + (lane >> 3); xoff[m] = (mt * 32 + row) * 1024 + (((lane & 7) ^ ((row >> 1) & 7)) << 4); }
        const u32 XR = L_P0X + w * 12288;
#pragma unroll
        for (int k = 0; k < 4; ++k) { const u32 row = (k >> 1) * 16 + t; ard[k] = XR + row * 128 + ((((k & 1) * 4 + g) ^ ((row >> 1) & 7)) << 4); }
        auto issue = [&](int st) {
            const char* sb = (const char*)c.x0 + ((st + rot) & 7) * 128;
#pragma unroll
            for (int m = 0; m < 4; ++m) glds(xoff[m], sb, XR + (st % 3) * 4096 + m * 1024);
        };
        issue_w(0); issue(0);
        STAMP(28);
        asm volatile("s_barrier" ::: "memory");
        issue(1); issue(2); issue_w(1);
        STAMP(29);
        asm volatile("s_waitcnt vmcnt(15)" ::: "memory");
        STAMP(1);
        bar();
        STAMP(2);
#pragma unroll
        for (int st = 0; st < 8; ++st) {
            if (st <= 2) asm volatile("s_waitcnt vmcnt(11)" ::: "memory");
            else if (st <= 5) asm volatile("s_waitcnt vmcnt(8)" ::: "memory");
            else if (st == 6) asm volatile("s_waitcnt vmcnt(4)" ::: "memory");
            else asm volatile("s_waitcnt vmcnt(0)" ::: "memory");
            if (st == 3) bar();
            u32x4 a[4];
#pragma unroll
            for (int k = 0; k < 4; ++k) a[k] = ldl<u32x4>(ard[k] + (st % 3) * 4096);
            if (st + 3 < 8) { asm volatile("s_waitcnt lgkmcnt(0)" : "+v"(a[0]), "+v"(a[1]), "+v"(a[2]), "+v"(a[3]) :: "memory"); issue(st + 3); }
            const u32 a0w = aw + ((st + rot) & 7) * 2048;
            kstep(a0w, a[0], a[2]); kstep(a0w + 1024, a[1], a[3]);
        }
        STAMP(3);
        c.ep = rfl(c.epv) + 1u;
        bar();
    } else {
        wait_lds(L_MISC + (M_XF + (w >> 1)) * 4, 1);
        STAMP(14);
        const u32 ab = WS_XF + ((mt * 32 + (g >> 1)) * 64 + (g & 1) * 32 + t) * 16;
        u32x4 a[32];
        auto ldx = [&](int k) { const u32 so = ((k + 2 * rot) & 15) * 2048; a[2 * k] = __builtin_amdgcn_raw_buffer_load_b128(c.rs, ab, so, 16); a[2 * k + 1] = __builtin_amdgcn_raw_buffer_load_b128(c.rs, ab + 256, so, 16); };
#pragma unroll
        for (int k = 0; k < PF3; ++k) ldx(k);
        MEMPIN();
#pragma unroll
        for (int k = 0; k < 16; ++k) {
            kstep(aw + ((k + 2 * rot) & 15) * 1024, a[2 * k], a[2 * k + 1]);
            if (k + PF3 < 16) { ldx(k + PF3); MEMPIN(); }
        }
        STAMP(15);
        wait_lds(L_MISC + M_STAT * 4, 4);
    }
    const u32 rec = ((w >> 2) * 32 + (w & 3)) * RS;
    const f32x4 csq = ldl<f32x4>(L_CS + g * 16), csk = ldl<f32x4>(L_CS + 64 + g * 16);
    const float csv = ldl<float>(L_CS + 128 + t * 4);
    float nn[4];
    u32 sel = 0;
    if (L == 1) sel = rfl(ldl<u32>(L_MISC + M_SEL * 4));
#pragma unroll
    for (int u = 0; u < 2; ++u) {
        const u32 ti = (w * 32 + 16 * u) * 4;
        const float cs = ldl<float>(L_C + ti + t * 4), cm = ldl<float>(L_CM + ti + t * 4);
        const f32x4 c4 = ldl<f32x4>(L_C + ti + g * 16), m4 = ldl<f32x4>(L_CM + ti + g * 16);
        const f32x4 q = (aq[u] * cs - csq * cm) * QSCALE, k = ak[u] * cs - csk * cm, v = av[u] * c4 - m4 * csv;
        u32x2 qp = {cvtpk(q[0], q[1]), cvtpk(q[2], q[3])}, kp = {cvtpk(k[0], k[1]), cvtpk(k[2], k[3])}, vp = {cvtpk(v[0], v[1]), cvtpk(v[2], v[3])};
        nn[u] = dot2(qp[0], qp[0], dot2(qp[1], qp[1], 0.f)); nn[2 + u] = dot2(kp[0], kp[0], dot2(kp[1], kp[1], 0.f));
        if (L == 1) {
            const u32 rel = sel - (u32)(mt * 32 + 16 * u + 4 * g);
            if (rel < 4u) { if (rel == 0) vp[0] &= 0xffff0000u; if (rel == 1) vp[0] &= 0xffffu; if (rel == 2) vp[1] &= 0xffff0000u; if (rel == 3) vp[1] &= 0xffffu; }
        }
        const u32 o = (g & 1) * 512 + (16 * u + t) * 16 + (g >> 1) * 8;
        stl<u32x2>(L_QX + w * 1024 + o, qp);
        stl<u32x2>(rec + o, kp);
        stl<u32x2>(rec + 1024 + u * 512 + (g & 1) * 256 + t * 16 + (g >> 1) * 8, vp);
    }
    float n1 = tr16(tr32(nn[0], nn[1]), tr32(nn[2], nn[3]));
    n1 = fmaxf(n1, dppf<0x128>(n1)); n1 = fmaxf(n1, dppf<0x124>(n1)); n1 = fmaxf(n1, dppf<0x122>(n1)); n1 = fmaxf(n1, dppf<0x121>(n1));
    n1 = swapmax(n1);
    if (!(n1 == n1)) n1 = INFINITY;
    if ((lane & 47) == 0) stl<float>(L_MISC + ((lane & 16) ? M_KM + w : M_QM + w) * 4, n1 * 1.01f);
    STAMP(L ? 12 : 27);
    bar();
}

template <int L>
DI void attn_phase(const Ctx& c) {
    const int w = c.w, lane = fresh_lane(), r = lane & 31, hh = lane >> 5;
    char* ws = c.ws;
    const int qp = w & 3, kh = w >> 2;
    bf16x8 qa = ldl<bf16x8>(L_QX + (2 * qp) * 1024 + lane * 16), qb = ldl<bf16x8>(L_QX + (2 * qp + 1) * 1024 + lane * 16);
    const float qm = fmaxf(ldl<float>(L_MISC + (M_QM + 2 * qp) * 4), ldl<float>(L_MISC + (M_QM + 2 * qp + 1) * 4));
    float km = 0.f;
#pragma unroll
    for (int i = 0; i < 4; ++i) km = fmaxf(km, ldl<float>(L_MISC + (M_KM + kh * 4 + i) * 4));
    f32x16 oa = {}, ob = {};
    float ma = 0.f, mb = 0.f;
    const u32 base = kh * 32 * RS;
    u32 ka = base + lane * 16;
    const bool one = r == 16 || r == 20;
    u32 v0a = base + (r < 16 ? 1024 + hh * 256 + r * 16 : (one ? 2048 + hh * 16 : 2112));
    u32 v1a = base + (r < 16 ? 1536 + hh * 256 + r * 16 : (one ? 2080 + hh * 16 : 2112));
    bool slow = !(rfl(__float_as_uint(qm * km)) <= __float_as_uint(BOUND2));
    int cnt = 32;
    STAMP(L ? 18 : 6); CSTAMP(L ? 18 : 6);
    if (slow) { ma = -INFINITY; mb = -INFINITY; }
    else {
#define FLIP 8
        int thr = 0;
        if (kh == 0) { __builtin_amdgcn_s_setprio(2); thr = FLIP; } else __builtin_amdgcn_s_setprio(1);
        thr = rfl(thr);
        u32 fa = L_MISC + (M_FLG + 32 * L + 1) * 4, fv, spin;
        asm volatile(ATTN_LOOP_ASM : "+v"(oa), "+v"(ob), "+v"(ka), "+v"(v0a), "+v"(v1a), "+s"(cnt), "+v"(fa), "=&s"(fv), "=&s"(spin)
                     : "v"(qa), "v"(qb), "s"(thr), "s"(SPIN_CAP << 4) : ATTN_LOOP_CLOBBERS, "scc", "memory");
        __builtin_amdgcn_s_setprio(0);
    }
#pragma nounroll
    for (; cnt > 0; --cnt) {
        if ((cnt & 3) == 0 && cnt < 32) wait_lds(L_MISC + (M_FLG + 32 * L + (32 - cnt) / 4) * 4, 1);
        tile_one<true>(ka, v0a, v1a, qa, oa, ma); tile_one<true>(ka, v0a, v1a, qb, ob, mb);
        ka += RS; v0a += RS; v1a += RS;
        slow = true;
    }
    if (L == 1) {
        wait_lds(L_MISC + M_MINI * 4, 1);
        STAMP(25);
        if (kh == 0) {
            const float kn = ldl<float>(L_MISC + M_KNEW * 4);
            const bool sl2 = slow || !(rfl(__float_as_uint(qm * kn)) <= __float_as_uint(BOUND2));
            const u32 k2 = L_MINI + lane * 16, v2 = L_MINI + (r < 16 ? 1024 + hh * 256 + r * 16 : (one ? 2048 + hh * 16 : 2112)), v3 = L_MINI + (r < 16 ? 1536 + hh * 256 + r * 16 : (one ? 2080 + hh * 16 : 2112));
            if (sl2) { tile_one<true>(k2, v2, v3, qa, oa, ma); tile_one<true>(k2, v2, v3, qb, ob, mb); }
            else { tile_one<false>(k2, v2, v3, qa, oa, ma); tile_one<false>(k2, v2, v3, qb, ob, mb); }
        }
    }
    STAMP(L ? 19 : 7); CSTAMP(L ? 19 : 7);
    bar();
    {
        const u32 sc = L_SCR + w * 2304 + lane * 4;
#pragma unroll
        for (int i = 0; i < 9; ++i) stl<float>(sc + i * 256, kh == 0 ? ob[i] : oa[i]);
        stl<float>(L_MX + w * 256 + lane * 4, kh == 0 ? mb : ma);
    }
    if (L == 0) STAMP(30);
    bar();
    if (L == 0) STAMP(31);
    {
        const u32 sc = L_SCR + (w ^ 4) * 2304 + lane * 4;
        const float mo = ldl<float>(L_MX + (w ^ 4) * 256 + lane * 4), mm = kh == 0 ? ma : mb;
        const float M = fmaxf(mo, mm), fo = __builtin_amdgcn_exp2f(mo - M), fm = __builtin_amdgcn_exp2f(mm - M);
        float o[9];
#pragma unroll
        for (int i = 0; i < 9; ++i) o[i] = (kh == 0 ? oa[i] : ob[i]) * fm + ldl<float>(sc + i * 256) * fo;
        const int mt = c.qr * 8 + 2 * qp + kh;
        if (L == 1) {
            const u32 sel = rfl(ldl<u32>(L_MISC + M_SEL * 4));
            if ((int)(sel >> 5) == mt && (u32)r == (sel & 31)) {
#pragma unroll
                for (int i = 0; i < 9; ++i) o[i] = ldl<float>(L_FIN + (hh * 9 + i) * 4);
            }
        }
        const float inv = 1.0f / o[8];
        u32x4 ov;
#pragma unroll
        for (int i = 0; i < 4; ++i) ov[i] = cvtpk(o[2 * i] * inv, o[2 * i + 1] * inv);
        st16(c.rs, WS_O + ((L * 64 + mt) * 32 + c.h) * 1024 + lane * 16, ov);
        vm0();
        if (arrive(M_OC, 8 * (L + 1)) && lane == 0) stf32(ws, WS_FO + ((L * 8 + c.qr) * 32 + c.h) * 128, c.ep);
    }
    STAMP(L ? 20 : 8);
}

template <int L>
DI void oproj_phase(const Ctx& c, u32x4 (&xk)[2]) {
    const int w = c.w, lane = fresh_lane(), r = lane & 31, hh = lane >> 5;
    char* ws = c.ws;
    const int m = w & 1, kh = (w >> 1) & 1;
    const int mt = c.qr * 8 + (c.h >> 3) * 2 + m, cb = c.h & 7;
    const int col0 = cb * 64 + kh * 32 + 4 * hh;
    const int tok = mt * 32 + r;
    f32x16 acc[2] = {};
    u32x2 x0v[4] = {}, wrv[4] = {};
    if (w < 4) {
#pragma unroll
        for (int i = 0; i < 4; ++i) { if (L == 0) x0v[i] = *(const GAS u32x2*)(c.x0 + tok * 512 + col0 + 8 * i); wrv[i] = *(const GAS u32x2*)(c.wr + col0 + 8 * i); }
        wait_lds(L_MISC + M_WP * 4, 4 * (L + 1));
        wait_lds(L_MISC + M_OF * 4, L + 1);
        STAMP(L ? 21 : 9);
        u32x4 a[16];
        const u32 ab = WS_O + ((L * 64 + mt) * 32 + kh * 16) * 1024 + lane * 16;
#pragma unroll
        for (int k = 0; k < 16; ++k) a[k] = ld16(c.rs, ab + k * 1024);
        MEMPIN();
        const u32 aw = L_WO + (kh * 256 + hh * 4 + ((lane & 15) >> 2)) * 64 + ((lane >> 4) & 1) * 32 + (lane & 3) * 8;
#pragma unroll
        for (int k = 0; k < 16; ++k) {
            acc[0] = MFMA(trfrag(aw + k * 1024, 512), __builtin_bit_cast(bf16x8, a[k]), acc[0]);
            acc[1] = MFMA(trfrag(aw + WO1 + k * 1024, 512), __builtin_bit_cast(bf16x8, a[k]), acc[1]);
        }
        STAMP(L ? 22 : 10);
        const u32 sc = L_SCR + w * 4096 + lane * 4;
#pragma unroll
        for (int i = 0; i < 16; ++i) stl<float>(sc + i * 256, kh == 0 ? acc[1][i] : acc[0][i]);
    }
    bar();
    if (w < 4) {
        float v[16];
        const u32 sc = L_SCR + (w ^ 2) * 4096 + lane * 4;
#pragma unroll
        for (int i = 0; i < 16; ++i) v[i] = (kh == 0 ? acc[0][i] : acc[1][i]) + ldl<float>(sc + i * 256);
        if (L == 0) {
#pragma unroll
            for (int i = 0; i < 4; ++i) { v[4 * i] += bflo(x0v[i][0]); v[4 * i + 1] += bfhi(x0v[i][0]); v[4 * i + 2] += bflo(x0v[i][1]); v[4 * i + 3] += bfhi(x0v[i][1]); }
        } else {
            const int tl = (c.h >> 3) * 64 + m * 32 + r;
            const float ra = ldl<float>(L_RA + tl * 4), rb = ldl<float>(L_RB + tl * 4);
            const u32 sel = rfl(ldl<u32>(L_MISC + M_SEL * 4));
            const bool issel = (u32)tok == sel;
#pragma unroll
            for (int i = 0; i < 4; ++i) {
                f32x4 x2 = {ra * bflo(xk[i >> 1][(i & 1) * 2]) + rb, ra * bfhi(xk[i >> 1][(i & 1) * 2]) + rb, ra * bflo(xk[i >> 1][(i & 1) * 2 + 1]) + rb, ra * bfhi(xk[i >> 1][(i & 1) * 2 + 1]) + rb};
                if (issel) x2 = ldl<f32x4>(F_X2 + (kh * 32 + 4 * hh + 8 * i) * 4);
#pragma unroll
                for (int k = 0; k < 4; ++k) v[4 * i + k] += x2[k];
            }
        }
#pragma unroll
        for (int i = 0; i < 8; ++i) xk[i >> 2][i & 3] = cvtpk(v[2 * i], v[2 * i + 1]);
        const int ksx = cb * 4 + kh * 2;
        st16(c.rs, WS_XF + ((L * 64 + mt) * 32 + ksx) * 1024 + lane * 16, xk[0]);
        st16(c.rs, WS_XF + ((L * 64 + mt) * 32 + ksx + 1) * 1024 + lane * 16, xk[1]);
        float p1 = 0.f, p2 = 0.f, p3 = 0.f;
#pragma unroll
        for (int i = 0; i < 8; ++i) { const u32 x = xk[i >> 2][i & 3]; p1 = dot2(x, 0x3f803f80u, p1); p2 = dot2(x, x, p2); p3 = dot2(x, wrv[i >> 1][i & 1], p3); }
        p1 = swapadd(p1); p2 = swapadd(p2); p3 = swapadd(p3);
        if (hh == 0) {
            const u32 pa = L_PS + kh * 768 + (m * 32 + r) * 4;
            stl<float>(pa, p1); stl<float>(pa + 256, p2); stl<float>(pa + 512, p3);
        }
        asm volatile("s_waitcnt lgkmcnt(0)" ::: "memory");
        if (lane == 0) __hip_atomic_fetch_add((LAS u32*)(size_t)(L_MISC + M_PS * 4), 1u, __ATOMIC_RELAXED, __HIP_MEMORY_SCOPE_WORKGROUP);
        vm0();
        if (arrive(M_XC, 4 * (L + 1)) && lane == 0) stf32(ws, WS_FX + ((L * 32 + c.qr * 4 + (c.h >> 3)) * 8 + cb) * 128, c.ep);
        STAMP(L ? 23 : 11);
    }
}

DI void final_phase(const Ctx& c, const u32x4 (&xk)[2]) {
    const int w = c.w, lane = fresh_lane(), r = lane & 31, hh = lane >> 5;
    char* ws = c.ws;
    if (w >= 4) return;
    const int m = w & 1, kh = (w >> 1) & 1;
    const int mt = c.qr * 8 + (c.h >> 3) * 2 + m, cb = c.h & 7;
    wait_lds(L_MISC + M_FIN * 4, 1);
    const float mu = ldl<float>(L_FIN + (m * 32 + r) * 4), rs = ldl<float>(L_FIN + 256 + (m * 32 + r) * 4);
    rsrc_t ro = __builtin_amdgcn_make_buffer_rsrc(c.out, 0, S_ * D_ * 2, 0x00020000);
    wait_lds(L_MISC + M_SEL2 * 4, 1);
    const u32 sel = rfl(ldl<u32>(L_MISC + M_SEL2 * 4)) - 1u;
#pragma unroll
    for (int f = 0; f < 2; ++f) {
        u32 o[4];
#pragma unroll
        for (int i = 0; i < 4; ++i) { float a = bflo(xk[f][i]), b = bfhi(xk[f][i]); o[i] = cvtpk(a + rbf((a - mu) * rs), b + rbf((b - mu) * rs)); }
        auto s0 = __builtin_amdgcn_permlane32_swap(o[0], o[2], false, false);
        auto s1 = __builtin_amdgcn_permlane32_swap(o[1], o[3], false, false);
        u32x4 ov = {s0[0], s1[0], s0[1], s1[1]};
        const u32 col = cb * 64 + kh * 32 + f * 16 + 8 * hh;
        if ((u32)(mt * 32 + r) != sel) __builtin_amdgcn_raw_buffer_store_b128(ov, ro, ((mt * 32 + r) * 512 + col) * 2, 0, 0);
    }
    STAMP(24);
}

DI void fetch_wo(const Ctx& c, int s, u32x4 (&v)[16]) {
    const int lane = fresh_lane();
    const rsrc_t rw = __builtin_amdgcn_make_buffer_rsrc((void*)c.wo, 0, D_ * D_ * 2, 0x00020000);
    const u32 vo = ((lane >> 3) * 512 + (lane & 7) * 8) * 2, so = (s * 128 * 512 + (c.h & 7) * 64) * 2;
#pragma unroll
    for (int i = 0; i < 16; ++i) { v[i] = __builtin_amdgcn_raw_buffer_load_b128(rw, vo, so + i * 8 * 512 * 2, 0); PACE(); }
}
DI void put_wo(int s, const u32x4 (&v)[16]) {
    const int lane = fresh_lane();
    const u32 a = L_WO + ((lane >> 2) & 1) * WO1 + (s * 128 + (lane >> 3)) * 64 + (lane & 3) * 16;
#pragma unroll
    for (int i = 0; i < 16; ++i) stl<u32x4>(a + i * 512, v[i]);
}
DI void fetch_w1(const Ctx& c, int s, u32x4 (&v)[12]) {
    const int lane = fresh_lane();
    const rsrc_t rw = __builtin_amdgcn_make_buffer_rsrc((void*)c.wqkv, 0, D_ * 1536 * 2, 0x00020000);
    const u32 vo = ((lane >> 1) * 1536 + (lane & 1) * 8) * 2;
#pragma unroll
    for (int i = 0; i < 12; ++i) { const int m = s * 12 + i; v[i] = __builtin_amdgcn_raw_buffer_load_b128(rw, vo, ((m & 15) * 32 * 1536 + (m >> 4) * 512 + c.h * 16) * 2, 0); PACE(); }
}
DI void put_w1(int s, const u32x4 (&v)[12]) {
    const int lane = fresh_lane();
#pragma unroll
    for (int i = 0; i < 12; ++i) stl<u32x4>(L_W1 + (s * 12 + i) * 1024 + lane * 16, v[i]);
}
template <int L> DI void init_pads(const Ctx& c) {
    const int lane = fresh_lane();
    if (lane < 40) {
        const u32 i = lane / 5, cc = lane % 5, x = cc < 4 ? 0x3f803f80u : 0u;
        u32x4 v = {x, x, x, x};
        if (L == 1) {
            const u32 sel = rfl(ldl<u32>(L_MISC + M_SEL * 4));
            const u32 rho = sel & 31, cs = (rho >> 4) * 2 + ((rho >> 2) & 1), jj = (rho & 3) + 4 * ((rho >> 3) & 1);
            if ((u32)(c.qr * 8) + i == (sel >> 5) && cc == cs) {
#pragma unroll
                for (int k = 0; k < 4; ++k) if ((u32)k == (jj >> 1)) v[k] &= (jj & 1) ? 0x0000ffffu : 0xffff0000u;
            }
        }
        stl<u32x4>(((i >> 2) * 32 + (i & 3)) * RS + 2048 + cc * 16, v);
    }
}
template <int L> DI void publish_chunk(const Ctx& c, int s) {
    const int lane = fresh_lane(); char* ws = c.ws;
    {
        u32x4 t[4];
#pragma unroll
        for (int i = 0; i < 2; ++i) { const int tl = 2 * s + i; const u32 rec = ((tl >> 2) * 32 + (tl & 3)) * RS + lane * 16; t[2 * i] = ldl<u32x4>(rec); t[2 * i + 1] = ldl<u32x4>(rec + 1024); }
        const u32 gk = WS_KV + ((L * 32 + c.h) * 64 + c.qr * 8 + 2 * s) * 2048 + lane * 16;
#pragma unroll
        for (int i = 0; i < 4; ++i) st16(c.rs, gk + i * 1024, t[i]);
        float kn = 0.f;
#pragma unroll
        for (int i = 0; i < 8; ++i) kn = fmaxf(kn, ldl<float>(L_MISC + (M_KM + i) * 4));
        vm0();
        if (arrive(M_KC, 4 * (L + 1)) && lane == 0) stf64(ws, WS_FKV + ((L * 32 + c.h) * 8 + c.qr) * 128, gran(c.ep, kn));
        STAMP(L ? 10 : 9);
    }
}
template <int L, int NT> DI void load_tiles(const Ctx& c, u32 slot, u32 t0, u32 need, float qall, u32 sel) {
    const int lane = fresh_lane(); char* ws = c.ws;
    const u32 FL = L_MISC + (M_FLG + 32 * L) * 4;
    const u32 ch = (c.qr + slot) & 7;
    const float kn = __uint_as_float(wait_gran(ws, WS_FKV + ((L * 32 + c.h) * 8 + ch) * 128, true, c.ep));
    if (slot == 1) STAMP(L ? 14 : 15);
    u32x4 t[2 * NT];
    const u32 gb = WS_KV + ((L * 32 + c.h) * 64 + t0) * 2048 + lane * 16;
#pragma unroll
    for (int i = 0; i < 2 * NT; ++i) t[i] = __builtin_amdgcn_raw_buffer_load_b128(c.rs, gb + i * 1024, ch * 16384, 16);
    MEMPIN();
    {
        const u32 i = lane >> 3, cc = lane & 7, ti = t0 + i, x = cc < 4 ? 0x3f803f80u : 0u;
        u32x4 v = {x, x, x, x};
        if (L == 1 && ch * 8 + ti == (sel >> 5)) {
            const u32 rho = sel & 31, cs = (rho >> 4) * 2 + ((rho >> 2) & 1), j2 = (rho & 3) + 4 * ((rho >> 3) & 1);
            if (cc == cs) {
#pragma unroll
                for (int k = 0; k < 4; ++k) if ((u32)k == (j2 >> 1)) v[k] &= (j2 & 1) ? 0x0000ffffu : 0xffff0000u;
            }
        }
        if (i < (u32)NT && cc < 5) stl<u32x4>(((ti >> 2) * 32 + slot * 4 + (ti & 3)) * RS + 2048 + cc * 16, v);
    }
#pragma unroll
    for (int i = 0; i < 2 * NT; ++i) { const u32 ti = t0 + (i >> 1); stl<u32x4>(((ti >> 2) * 32 + slot * 4 + (ti & 3)) * RS + (i & 1) * 1024 + lane * 16, t[i]); }
    asm volatile("s_waitcnt lgkmcnt(0)" ::: "memory");
    const bool safe = __float_as_uint(qall * kn) <= __float_as_uint(BOUND2);
    u32 old = need - 1;
    if (need > 1 && lane == 0) old = __hip_atomic_fetch_add((LAS u32*)(size_t)(FL + M_NRD * 4 + slot * 4), 1u, __ATOMIC_RELAXED, __HIP_MEMORY_SCOPE_WORKGROUP);
    if (lane == 0 && old == need - 1) stl<u32>(FL + slot * 4, safe ? 1u : 2u);
    if (slot == 1) STAMP(L ? 12 : 11);
}
template <int L> DI void load_slots(const Ctx& c, int s, int part) {
    float qall = 0.f;
#pragma unroll
    for (int i = 0; i < 8; ++i) qall = fmaxf(qall, ldl<float>(L_MISC + (M_QM + i) * 4));
    u32 sel = 0;
    if (L == 1) sel = rfl(ldl<u32>(L_MISC + M_SEL * 4));
    if (part == 0) { load_tiles<L, 2>(c, 1, 2 * s, 4, qall, sel); load_tiles<L, 8>(c, 2 + s, 0, 1, qall, sel); }
    else if (s < 2) load_tiles<L, 8>(c, 6 + s, 0, 1, qall, sel);
}

DI void stats0(const Ctx& c, int s) {
    const int lane = fresh_lane();
    if (s == 0) {
        u32x4 a[8];
#pragma unroll
        for (int i = 0; i < 8; ++i) a[i] = *(const GAS u32x4*)(c.x0 + (c.qr * 256 + c.h * 8 + i) * 512 + lane * 8);
        float s1 = 0.f, s2 = 0.f;
#pragma unroll
        for (int i = 0; i < 8; ++i) {
            float a1 = 0.f, a2 = 0.f;
#pragma unroll
            for (int k = 0; k < 4; ++k) { a1 = dot2(a[i][k], 0x3f803f80u, a1); a2 = dot2(a[i][k], a[i][k], a2); }
            a1 = wave_sum(a1); a2 = wave_sum(a2);
            if (lane == i) { s1 = a1; s2 = a2; }
        }
        const float mean = s1 * (1.f / 512.f), var = fmaxf(s2 * (1.f / 512.f) - mean * mean, 0.f), cs = __builtin_amdgcn_rsqf(var + EPS);
        if (lane < 8) st16(c.rs, WS_ST0 + (c.qr * 32 + c.h) * 128 + lane * 16, (u32x4){__float_as_uint(cs), c.ep, __float_as_uint(cs * mean), c.ep});
    }
    if (s == 0) __builtin_amdgcn_s_sleep(8); else __builtin_amdgcn_s_sleep(24);
    const u32x4 q = wait_rec<false>(c.rs, WS_ST0 + (c.qr * 32 + s * 8 + (lane >> 3)) * 128 + (lane & 7) * 16, c.ep);
    stl<u32>(L_C + (s * 64 + lane) * 4, q[0]); stl<u32>(L_CM + (s * 64 + lane) * 4, q[2]);
}
DI void stats1(const Ctx& c, int s) {
    const int lane = fresh_lane();
    const u32x4 q = wait_rec<false>(c.rs, WS_ST1 + (c.qr * 256 + s * 64 + lane) * 16, c.ep);
    const float mean = __uint_as_float(q[0]), var = __uint_as_float(q[2]);
    const float rs1 = __builtin_amdgcn_rsqf(var + EPS), a = 1.f + rs1, cs = a * __builtin_amdgcn_rsqf(a * a * var + EPS);
    const u32 o = (s * 64 + lane) * 4;
    stl<float>(L_C + o, cs); stl<float>(L_CM + o, cs * mean); stl<float>(L_RA + o, a); stl<float>(L_RB + o, -mean * rs1);
}
template <int L> DI void stats_exchange(const Ctx& c) {
    const int lane = fresh_lane();
    const int g = c.qr * 4 + (c.h >> 3), cb = c.h & 7;
    u32x4 wv = {};
    if (cb == 0) wv = *(const GAS u32x4*)(c.wr + lane * 8);
    wait_lds(L_MISC + M_PS * 4, 4 * (L + 1));
    f32x4 p;
#pragma unroll
    for (int k = 0; k < 3; ++k) p[k] = ldl<float>(L_PS + k * 256 + lane * 4) + ldl<float>(L_PS + 768 + k * 256 + lane * 4);
    const u32 pb = WS_PST + ((L * 32 + g) * 8) * 1024 + lane * 16;
    { u32x4 v = __builtin_bit_cast(u32x4, p); v[3] = c.ep; st16(c.rs, pb + cb * 1024, v); }
    if (L == 0 && cb != 0) return;
    __builtin_amdgcn_s_sleep(8);
    u32x4 q[8];
#pragma nounroll
    for (u32 sp = 0; sp < SPIN_CAP; ++sp) {
        bool ok = true;
#pragma unroll
        for (int b = 0; b < 8; ++b) { q[b] = ld16(c.rs, pb + b * 1024); ok &= q[b][3] == c.ep; }
        if (__all(ok)) break;
        __builtin_amdgcn_s_sleep(1);
    }
    float t1 = 0.f, t2 = 0.f, t3 = 0.f;
#pragma unroll
    for (int b = 0; b < 8; ++b) { t1 += __uint_as_float(q[b][0]); t2 += __uint_as_float(q[b][1]); t3 += __uint_as_float(q[b][2]); }
    const float mu = t1 * (1.f / 512.f), var = fmaxf(t2 * (1.f / 512.f) - mu * mu, 0.f), rs = __builtin_amdgcn_rsqf(var + EPS);
    if (L == 0) st16(c.rs, WS_ST1 + (g * 64 + lane) * 16, (u32x4){__float_as_uint(mu), c.ep, __float_as_uint(var), c.ep});
    else {
        stl<float>(L_FIN + lane * 4, mu); stl<float>(L_FIN + 256 + lane * 4, rs);
        asm volatile("s_waitcnt lgkmcnt(0)" ::: "memory");
        if (lane == 0) stl<u32>(L_MISC + M_FIN * 4, 1u);
    }
    if (cb == 0) {
        float sw = 0.f;
#pragma unroll
        for (int i = 0; i < 4; ++i) sw = dot2(wv[i], 0x3f803f80u, sw);
        sw = wave_sum(sw);
        float rr = rs * (t3 - mu * sw);
        if (!(rr == rr)) rr = -INFINITY;
        const float best = wave_max(rr);
        const u64 bal = __ballot(rr == best);
        const int bl = rfl(__builtin_ctzll(bal));
        const u32 bmu = __builtin_amdgcn_readlane(__float_as_uint(mu), bl), brs = __builtin_amdgcn_readlane(__float_as_uint(rs), bl);
        if (lane < 16) {
            const u32x4 v = lane & 1 ? (u32x4){bmu, c.ep, brs, c.ep} : (u32x4){__float_as_uint(best), c.ep, (u32)(g * 64 + bl), c.ep};
            st16(c.rs, WS_CAND + ((L * 8 + (lane >> 1)) * 32 + g) * 128 + (lane & 1) * 16, v);
        }
    }
}
template <int L> DI void get_sel(const Ctx& c) {
    const int lane = fresh_lane(), g = lane & 31;
    const u32x4 q = wait_rec<false>(c.rs, WS_CAND + ((L * 8 + (c.bid & 7)) * 32 + g) * 128 + (lane >> 5) * 16, c.ep);
    const float rr = lane < 32 ? __uint_as_float(q[0]) : -INFINITY;
    const float best = wave_max(rr);
    const u64 bal = __ballot(lane < 32 && rr == best);
    const int bu = rfl(bal ? __builtin_ctzll(bal) : 0);
    const u32 sel = __builtin_amdgcn_readlane(q[2], bu) & 2047u, mu = __builtin_amdgcn_readlane(q[0], bu + 32), rs = __builtin_amdgcn_readlane(q[2], bu + 32);
    if (L == 1) wait_flags32(c.ws, WS_FX + ((32 + (sel >> 6)) * 8 + (lane & 7)) * 128, true, c.ep);
    if (lane == 0) {
        stl<float>(L_MISC + M_RV * 4, best); stl<u32>(L_MISC + (M_RV + 1) * 4, mu); stl<u32>(L_MISC + (M_RV + 2) * 4, rs);
        if (L == 0) stl<u32>(L_MISC + M_SEL * 4, sel);
        asm volatile("s_waitcnt lgkmcnt(0)" ::: "memory");
        if (L == 1) stl<u32>(L_MISC + M_SEL2 * 4, sel + 1u);
    }
    asm volatile("s_waitcnt lgkmcnt(0)" ::: "memory");
}
template <int L> DI void wait_xtiles(const Ctx& c, u32 g) {
    const int lane = fresh_lane();
    wait_flags32(c.ws, WS_FX + ((L * 32 + g) * 8 + (lane & 7)) * 128, true, c.ep);
}

template <int NS> DI void gemv_acc(const u32x2 (&wv)[NS], u32 xa, float (&acc)[4]) {
#pragma unroll
    for (int i = 0; i < NS; ++i) {
        u32x2 x = ldl<u32x2>(xa + i * 128);
        acc[0] = dot2(wv[i][0], x[0], acc[0]); acc[1] = dot2(wv[i][0], x[1], acc[1]);
        acc[2] = dot2(wv[i][1], x[0], acc[2]); acc[3] = dot2(wv[i][1], x[1], acc[3]);
    }
}
DI float red4(const float (&a)[4]) { float v = tr16(tr32(a[0], a[1]), tr32(a[2], a[3])); v += dppf<0x128>(v); return v + dppf<0x124>(v); }
DI bool last_arriver(u32 round, int lane) {
    asm volatile("s_waitcnt lgkmcnt(0)" ::: "memory");
    u32 old = 0;
    if (lane == 0) old = __hip_atomic_fetch_add((LAS u32*)(size_t)(L_MISC + M_CNT * 4), 1u, __ATOMIC_RELAXED, __HIP_MEMORY_SCOPE_WORKGROUP);
    old = rfl(old);
    asm volatile("" ::: "memory");
    return old == round * 4 + 3;
}

DI u32x2 ld8(rsrc_t r, u32 voff, u32 soff) { return __builtin_amdgcn_raw_buffer_load_b64(r, voff, soff, 0); }
DI void ffn_fetch(const Ctx& c, int s, u32x2 (&wb)[16]) {
    const int lane = fresh_lane(), bid = c.bid;
    const bool gu = bid < 112;
    const rsrc_t ra = __builtin_amdgcn_make_buffer_rsrc((void*)(gu ? c.wg : c.wd), 0, D_ * F_ * 2, 0x00020000), rb = __builtin_amdgcn_make_buffer_rsrc((void*)(gu ? c.wu : c.wd), 0, D_ * F_ * 2, 0x00020000);
    const u32 vo = gu ? ((lane >> 2) * F_ + (lane & 3) * 4) * 2 : ((lane >> 1) * D_ + (lane & 1) * 4) * 2;
    const u32 so = gu ? (s * 128 * F_ + bid * 16) * 2 : (s * 448 * D_ + ((bid - 112) & 63) * 8) * 2;
    const u32 st = gu ? 16 * F_ * 2 : 32 * D_ * 2;
    if (bid < 176) {
#pragma unroll
        for (int i = 0; i < 8; ++i) { wb[i] = ld8(ra, vo, so + i * st); PACE(); wb[8 + i] = ld8(rb, vo, so + (gu ? i : (i < 6 ? 8 + i : 13)) * st); PACE(); }
    } else {
#pragma unroll
        for (int i = 0; i < 16; ++i) wb[i] = (u32x2){0u, 0u};
    }
    MEMPIN();
}
template <int L, int PART> DI void ffn_chain(const Ctx& c, int s, u32& round, const u32x2 (&wb)[16]) {
    const int lane = fresh_lane(), bid = c.bid; char* ws = c.ws;
    const u32 ep = c.ep;
    auto routed = [&](u32& sel, float& rv, float& mu, float& rs) {
        if (L == 1) wait_lds(L_MISC + M_SEL2 * 4, 1);
        sel = L ? rfl(ldl<u32>(L_MISC + M_SEL2 * 4)) - 1u : rfl(ldl<u32>(L_MISC + M_SEL * 4));
        rv = ldl<float>(L_MISC + M_RV * 4); mu = ldl<float>(L_MISC + (M_RV + 1) * 4); rs = ldl<float>(L_MISC + (M_RV + 2) * 4);
    };
    const u32 ridx = ((lane >> 5) & 1) + 2 * ((lane >> 4) & 1);
    const bool rown = (lane & 12) == 0;
    if (bid < 112) {
        if (PART == 1) return;
        const int rg = lane >> 2, cq = lane & 3;
        u32x2 wg[8], wu[8];
#pragma unroll
        for (int i = 0; i < 8; ++i) { wg[i] = wb[i]; wu[i] = wb[8 + i]; }
        u32 sel; float rv, mu, rs;
        routed(sel, rv, mu, rs);
        {
            const u32 ks = s * 8 + (lane >> 3), h2 = (lane >> 2) & 1, pr = lane & 3;
            const u32 xw = __builtin_amdgcn_raw_buffer_load_b32(c.rs, WS_XF + (((L * 64 + (sel >> 5)) * 32 + ks) * 64 + h2 * 32 + (sel & 31)) * 16 + pr * 4, 0, 16);
            const u32 b = cvtpk((bflo(xw) - mu) * rs, (bfhi(xw) - mu) * rs);
            const u32 k0 = ks * 16 + 8 * (pr >> 1) + 4 * h2 + 2 * (pr & 1);
            stl<u32x4>(F_XN + k0 * 8, (u32x4){b & 0xffffu, b << 16, b >> 16, b & 0xffff0000u});
        }
        if (L == 0) STAMP(18);
        float ag[4] = {}, au[4] = {};
        gemv_acc<8>(wg, F_XN + (s * 128 + rg) * 8, ag); gemv_acc<8>(wu, F_XN + (s * 128 + rg) * 8, au);
        const float g1 = red4(ag), u1 = red4(au);
        if (rown) { stl<float>(F_RED + (s * 32 + cq * 4 + ridx) * 4, g1); stl<float>(F_RED + (s * 32 + 16 + cq * 4 + ridx) * 4, u1); }
        if (last_arriver(round, lane) && lane < 16) {
            float g = 0.f, u = 0.f;
#pragma unroll
            for (int k = 0; k < 4; ++k) { g += ldl<float>(F_RED + (k * 32 + lane) * 4); u += ldl<float>(F_RED + (k * 32 + 16 + lane) * 4); }
            g = rbf(g); u = rbf(u);
            const float sg = rbf(g * __builtin_amdgcn_rcpf(1.f + __expf(-g)));
            stf64(ws, WS_HG + (L * 1792 + bid * 16 + lane) * 8, gran(ep, cvtpk(sg * u, 0.f) & 0xffffu));
        }
        if (L == 0) STAMP(19);
        ++round;
    } else if (PART == 0) { return;
    } else if (bid < 176) {
        const int j = bid - 112;
        const int rg = lane >> 1, cq = lane & 1;
        u32 sel; float rv, mu, rs;
        routed(sel, rv, mu, rs);
        const u32 d = (j & 1) * 8 + (lane & 7), h2 = (d >> 2) & 1, jj = (d & 3) + 4 * (d >> 3);
        const u32 xw = __builtin_amdgcn_raw_buffer_load_b32(c.rs, WS_XF + ((((L * 64 + (sel >> 5)) * 32 + (j >> 1)) * 64 + h2 * 32 + (sel & 31)) * 8 + (jj & ~1u)) * 2, 0, 16);
        u32 hq[7];
        sweep_grans<7, 512, L ? 1 : 6>(c.rs, WS_HG + (L * 1792 + s * 448 + lane) * 8, ep, hq);
        if (L == 0) STAMP(20);
        float acc[4] = {};
#pragma unroll
        for (int part = 0; part < 4; ++part) {
#pragma unroll
            for (int i = 0; i < 2; ++i) if (2 * part + i < 7) stl<u32x2>(F_XN + (s * 128 + i * 64 + lane) * 8, (u32x2){hq[2 * part + i], hq[2 * part + i] << 16});
#pragma unroll
            for (int i = 0; i < 4; ++i) if (part * 4 + i < 14) {
                u32x2 x = ldl<u32x2>(F_XN + (s * 128 + i * 32 + rg) * 8);
                const u32x2 wv = wb[part * 4 + i];
                acc[0] = dot2(wv[0], x[0], acc[0]); acc[1] = dot2(wv[0], x[1], acc[1]); acc[2] = dot2(wv[1], x[0], acc[2]); acc[3] = dot2(wv[1], x[1], acc[3]);
            }
        }
        float v = red4(acc); v += dppf<0x122>(v);
        if ((lane & 14) == 0) stl<float>(F_RED + (s * 32 + cq * 4 + ridx) * 4, v);
        if (last_arriver(round, lane) && lane < 8) {
            float y = 0.f;
#pragma unroll
            for (int k = 0; k < 4; ++k) y += ldl<float>(F_RED + (k * 32 + lane) * 4);
            const float sg = rbf(__builtin_amdgcn_rcpf(1.f + __expf(-rbf(rv))));
            y = rbf(rbf(y) * sg);
            const float x = (jj & 1) ? bfhi(xw) : bflo(xw);
            const float o = rbf(x + rbf(rbf((x - mu) * rs) + y));
            if (L == 0) stf64(ws, WS_X2S + j * 128 + lane * 8, gran(ep, o));
            else __hip_atomic_store((GAS u16*)(c.out + sel * 512 + j * 8 + lane), (u16)(__float_as_uint(o) >> 16), __ATOMIC_RELAXED, __HIP_MEMORY_SCOPE_AGENT);
        }
        if (L == 0) STAMP(21);
        ++round;
    }
    if (L == 0 && bid >= 160) {
        const int j = bid - 160;
        const int rg = lane >> 2, cq = lane & 3;
        const rsrc_t rq_ = __builtin_amdgcn_make_buffer_rsrc((void*)c.wqkv, 0, D_ * 1536 * 2, 0x00020000);
        u32x2 wq[8];
        const u32 vo = (rg * 1536 + cq * 4) * 2, so = (s * 128 * 1536 + j * 16) * 2;
#pragma unroll
        for (int i = 0; i < 8; ++i) wq[i] = ld8(rq_, vo, so + i * 16 * 1536 * 2);
        MEMPIN();
        if (s == 0) {
            u32 xq[8];
            sweep_grans<8, 1024, 6>(c.rs, WS_X2S + (lane >> 3) * 128 + (lane & 7) * 8, ep, xq);
            STAMP(22);
            float a1 = 0.f, a2 = 0.f;
#pragma unroll
            for (int i = 0; i < 8; ++i) { const float x = __uint_as_float(xq[i]); a1 += x; a2 += x * x; }
            a1 = wave_sum(a1); a2 = wave_sum(a2);
            const float m2 = a1 * (1.f / 512.f), r2 = __builtin_amdgcn_rsqf(fmaxf(a2 * (1.f / 512.f) - m2 * m2, 0.f) + EPS);
#pragma unroll
            for (int i = 0; i < 8; ++i) { const u32 b = cvtpk((__uint_as_float(xq[i]) - m2) * r2, 0.f) & 0xffffu; stl<u32x2>(F_XN + (i * 64 + lane) * 8, (u32x2){b, b << 16}); }
            asm volatile("s_waitcnt lgkmcnt(0)" ::: "memory");
            if (lane == 0) stl<u32>(L_MISC + M_XN * 4, 1u);
        } else wait_lds_slow(L_MISC + M_XN * 4, 1);
        float acc[4] = {};
        gemv_acc<8>(wq, F_XN + (s * 128 + rg) * 8, acc);
        const float v = red4(acc);
        if (rown) stl<float>(F_RED + (s * 32 + cq * 4 + ridx) * 4, v);
        if (last_arriver(round, lane) && lane < 16) {
            float y = 0.f;
#pragma unroll
            for (int k = 0; k < 4; ++k) y += ldl<float>(F_RED + (k * 32 + lane) * 4);
            if (j < 32) y *= QSCALE;
            stf64(ws, WS_QKVS + (j * 16 + lane) * 8, gran(ep, cvtpk(y, 0.f) & 0xffffu));
        }
        STAMP(23);
        ++round;
    }
}
DI void build_mini(const Ctx& c, u32 sel) {
    const int lane = fresh_lane(); char* ws = c.ws;
#pragma unroll
    for (int i = 0; i < 3; ++i) if (i * 64 + lane < (int)(RS / 16)) stl<u32x4>(L_MINI + (i * 64 + lane) * 16, (u32x4){0, 0, 0, 0});
    const int part = lane >> 4, d = lane & 15;
    const u32 v = wait_gran(ws, WS_QKVS + ((part < 3 ? part : 0) * 512 + c.h * 16 + d) * 8, true, c.ep);
    const u32 slot = ((d >> 2) & 1) * 8 + (d & 3) + 4 * (d >> 3);
    if (part == 0) stl<u16>(F_QS + slot * 2, (u16)v);
    if (part == 1) stl<u16>(L_MINI + (slot >> 3) * 512 + (slot & 7) * 2, (u16)v);
    if (part == 2) stl<u16>(L_MINI + 1024 + d * 16, (u16)v);
    if (lane == 0) stl<u16>(L_MINI + 2048, (u16)0x3f80);
    float kk = part == 1 ? bflo(v) * bflo(v) : 0.f;
    kk = wave_sum(kk) * 1.02f;
    if (lane == 0) stl<float>(L_MISC + M_KNEW * 4, kk);
    const int g = c.qr * 4 + (c.h >> 3);
    if ((int)(sel >> 6) == g) { const u32 x = wait_gran(ws, WS_X2S + ((c.h & 7) * 8 + (lane >> 3)) * 128 + (lane & 7) * 8, true, c.ep); stl<u32>(F_X2 + lane * 4, x); }
    asm volatile("s_waitcnt lgkmcnt(0)" ::: "memory");
    if (lane == 0) stl<u32>(L_MISC + M_MINI * 4, 1u);
}

DI void touch(const void* base, u32 first, u32 n) {
    const u32 ln = fresh_lane();
    for (u32 i = 0; i < n; i += 64) glds4((first + (i + ln < n ? i + ln : 0u)) * 64, base, L_FIN);
}
constexpr u32 R_P = L_PS, R_PART = L_PS + 768, R_MRG = L_PS + 1088, R_ALL = L_PS + 1168;
DI void routed_query(const Ctx& c, int s, u32 sel, u32& round) {
    const int lane = fresh_lane(), r = lane & 31, hh = lane >> 5;
    const bf16x8 qs = ldl<bf16x8>(F_QS + hh * 16);
    const bool own = (int)(sel >> 8) == c.qr, one = r == 16 || r == 20;
    float sc[3];
    float mx = -INFINITY;
#pragma unroll
    for (int t = 0; t < 3; ++t) {
        const u32 ti = 2 * s + t, rec = t < 2 ? ((ti >> 2) * 32 + (ti & 3)) * RS : L_MINI;
        const f32x16 z = {};
        const f32x16 sv = MFMA(qs, ldl<bf16x8>(rec + lane * 16), z);
        const bool valid = t < 2 ? (u32)(c.qr * 256 + ti * 32 + r) != sel : (own && s == 0 && r == 0);
        sc[t] = valid ? sv[0] : -INFINITY;
        mx = fmaxf(mx, sc[t]);
    }
    mx = wave_max(mx);
    const u32 pa = R_P + s * 192 + (r >> 4) * 32 + ((r >> 2) & 1) * 16 + ((r & 3) + 4 * ((r >> 3) & 1)) * 2;
#pragma unroll
    for (int t = 0; t < 3; ++t) {
        const float p = __builtin_amdgcn_exp2f(sc[t] - mx);
        if (hh == 0) stl<u16>(pa + t * 64, (u16)(cvtpk(p, p) & 0xffffu));
    }
    f32x16 os = {};
#pragma unroll
    for (int t = 0; t < 3; ++t) {
        const u32 ti = 2 * s + t, rec = t < 2 ? ((ti >> 2) * 32 + (ti & 3)) * RS : L_MINI;
        const bf16x8 v0 = ldl<bf16x8>(rec + (r < 16 ? 1024 + hh * 256 + r * 16 : (one ? 2048 + hh * 16 : 2112))), v1 = ldl<bf16x8>(rec + (r < 16 ? 1536 + hh * 256 + r * 16 : (one ? 2080 + hh * 16 : 2112)));
        const bf16x8 p0 = ldl<bf16x8>(R_P + s * 192 + t * 64 + hh * 16), p1 = ldl<bf16x8>(R_P + s * 192 + t * 64 + 32 + hh * 16);
        os = MFMA(v0, p0, os); os = MFMA(v1, p1, os);
    }
    if (r == 0) {
#pragma unroll
        for (int i = 0; i < 9; ++i) stl<float>(R_PART + ((s * 2 + hh) * 10 + i) * 4, os[i]);
        stl<float>(R_PART + ((s * 2 + hh) * 10 + 9) * 4, mx);
    }
    if (last_arriver(round, lane)) {
        if (lane < 18) {
            const int h2 = lane >= 8 && lane < 16, i = lane < 16 ? (lane & 7) : 8;
            float M = -INFINITY, a = 0.f;
#pragma unroll
            for (int k = 0; k < 4; ++k) M = fmaxf(M, ldl<float>(R_PART + (k * 20 + 9) * 4));
#pragma unroll
            for (int k = 0; k < 4; ++k) a += ldl<float>(R_PART + ((k * 2 + h2) * 10 + i) * 4) * __builtin_amdgcn_exp2f(ldl<float>(R_PART + (k * 20 + 9) * 4) - M);
            stl<float>(R_MRG + lane * 4, lane == 17 ? M : a);
        }
        if (lane < 6) st16(c.rs, WS_RQ + (c.h * 8 + c.qr) * 128 + lane * 16, (u32x4){ldl<u32>(R_MRG + lane * 12), ldl<u32>(R_MRG + lane * 12 + 4), ldl<u32>(R_MRG + lane * 12 + 8), c.ep});
        if (own) {
            const u32 b = lane / 6, k = lane % 6;
            const u32x4 q = wait_rec<true>(c.rs, WS_RQ + (c.h * 8 + (lane < 48 ? b : 0)) * 128 + (lane < 48 ? k : 0) * 16, c.ep);
            if (lane < 48) { stl<u32>(R_ALL + (b * 18 + k * 3) * 4, q[0]); stl<u32>(R_ALL + (b * 18 + k * 3 + 1) * 4, q[1]); stl<u32>(R_ALL + (b * 18 + k * 3 + 2) * 4, q[2]); }
            if (lane < 18) {
                const int h2 = lane / 9, i = lane % 9, e = i < 8 ? h2 * 8 + i : 16;
                float M = -INFINITY, a = 0.f;
#pragma unroll
                for (int k2 = 0; k2 < 8; ++k2) M = fmaxf(M, ldl<float>(R_ALL + (k2 * 18 + 17) * 4));
#pragma unroll
                for (int k2 = 0; k2 < 8; ++k2) a += ldl<float>(R_ALL + (k2 * 18 + e) * 4) * __builtin_amdgcn_exp2f(ldl<float>(R_ALL + (k2 * 18 + 17) * 4) - M);
                stl<float>(L_FIN + (h2 * 9 + i) * 4, a);
            }
        }
    }
    ++round;
}
DI void poll32(const Ctx& c, u32 off, u32 word, u32 val) {
    const int lane = fresh_lane();
    wait_flags32(c.ws, off + (lane & 31) * 128, true, c.ep);
    if (lane == 0) stl<u32>(L_MISC + word * 4, val);
    asm volatile("s_waitcnt lgkmcnt(0)" ::: "memory");
}
DI void touch_pages(const Ctx& c) {
    const u32 idx = (u32)(c.bid >> 3) * 16 + (fresh_lane() & 15);
    const u64 b0 = (u64)c.ws, b1 = (u64)c.out - 288ull * 65536, b2 = (u64)c.wo - 320ull * 65536, b3 = (u64)c.wg - 328ull * 65536, b4 = (u64)c.wu - 356ull * 65536,
              b5 = (u64)c.wd - 384ull * 65536;
    u64 p = b0;
    p = idx >= 288 ? b1 : p; p = idx >= 320 ? b2 : p; p = idx >= 328 ? b3 : p; p = idx >= 356 ? b4 : p; p = idx >= 384 ? b5 : p;
    p += (u64)idx * 65536;
    p = idx >= 412 ? (u64)c.wr : p;
    u32 keep;
    asm volatile("s_mov_b32 %0, m0\n\ts_mov_b32 m0, %2\n\ts_nop 0\n\tglobal_load_lds_dword %1, off sc1\n\ts_mov_b32 m0, %0" : "=&s"(keep) : "v"(p), "s"(L_FIN) : "memory");
}
DI void side_program(Ctx& c) {
    const int s = c.w - 8;
    u32 round = 0;
    __builtin_amdgcn_s_setprio(3);
    if (s == 0) { const u32 ln = fresh_lane(); stl<u32>(L_MISC + ln * 4, 0u); stl<u32>(L_MISC + 256 + ln * 4, (ln & 31) == 8 ? 1u : 0u); }
    asm volatile("s_barrier" ::: "memory");
    c.ep = rfl(c.epv) + 1u;
    stats0(c, s);
    STAMP(1);
    bar();
    bar();
    {
        const int lane = fresh_lane(), t = lane & 15, g = lane >> 4;
        const u32 aw = L_P0W + (16 * (g >> 1) + 4 * (g & 1) + (t >> 2)) * 32 + (t & 3) * 8;
        float a[3] = {};
#pragma unroll
        for (int k = 0; k < 4; ++k)
#pragma unroll
            for (int n = 0; n < 3; ++n) {
                const u32x4 wv = __builtin_bit_cast(u32x4, trfrag(aw + n * 16384 + (s * 4 + k) * 1024, 256));
#pragma unroll
                for (int i = 0; i < 4; ++i) a[n] = dot2(wv[i], 0x3f803f80u, a[n]);
            }
#pragma unroll
        for (int n = 0; n < 3; ++n) { a[n] = swapadd(swap16add(a[n])); if (lane < 16) stl<float>(L_PS + (s * 64 + n * 16 + lane) * 4, a[n]); }
        if (last_arriver(round, lane) && lane < 48) stl<float>(L_CS + lane * 4, ldl<float>(L_PS + lane * 4) + ldl<float>(L_PS + 256 + lane * 4) + ldl<float>(L_PS + 512 + lane * 4) + ldl<float>(L_PS + 768 + lane * 4));
        ++round;
    }
    STAMP(2);
    bar();
    if (s == 0) init_pads<0>(c);
    bar();
    publish_chunk<0>(c, s);
    __builtin_amdgcn_s_sleep(4);
    load_slots<0>(c, s, 0); load_slots<0>(c, s, 1);
    STAMP(5);
    u32x2 wb[16];
    ffn_fetch(c, s, wb);
    {
        u32x4 wo[16], w1[12];
        fetch_wo(c, s, wo); fetch_w1(c, s, w1);
        bar();
        bar();
        wait_lds(L_MISC + M_OC * 4, 8);
        put_wo(s, wo); put_w1(s, w1);
        lds_inc(M_WP);
    }
    if (s == 0) poll32(c, WS_FO + (0 * 8 + c.qr) * 32 * 128, M_OF, 1);
    bar();
    if (s == 0) {
        __builtin_amdgcn_s_sleep(8);
        const int lane = fresh_lane();
        u32 done = 0;
#pragma nounroll
        for (u32 sp = 0; sp < SPIN_CAP && done != 15u; ++sp) {
            const u32 v = ldf32(c.ws, WS_FX + (c.qr * 32 + (lane & 31)) * 128);
            const u32 ok = (u32)__ballot(v == c.ep);
            u32 now = 0;
#pragma unroll
            for (int gq = 0; gq < 4; ++gq) if (((ok >> (8 * gq)) & 255u) == 255u) now |= 1u << gq;
            if (lane < 4 && ((now & ~done) >> lane & 1u)) stl<u32>(L_MISC + (M_XF + lane) * 4, 1u);
            done |= now;
            if (done != 15u) __builtin_amdgcn_s_sleep(1);
        }
    }
    if (s == 1) stats_exchange<0>(c);
    STAMP(7);
    stats1(c, s);
    STAMP(8);
    if (s == 1) {
        get_sel<0>(c);
        wait_xtiles<0>(c, rfl(ldl<u32>(L_MISC + M_SEL * 4)) >> 6);
        init_pads<1>(c);
    }
    lds_inc(M_STAT);
    STAMP(13);
    bar();
    const u32 sel = rfl(ldl<u32>(L_MISC + M_SEL * 4));
    publish_chunk<1>(c, s);
    __builtin_amdgcn_s_sleep(4);
    load_slots<1>(c, s, 0);
    ffn_chain<0, 0>(c, s, round, wb);
    load_slots<1>(c, s, 1);
    STAMP(16);
    ffn_chain<0, 1>(c, s, round, wb);
    if (s == 0) build_mini(c, sel);
    STAMP(17);
    wait_lds(L_MISC + M_MINI * 4, 1);
    routed_query(c, s, sel, round);
    {
        u32x4 wo[16];
        fetch_wo(c, s, wo);
        bar();
        bar();
        wait_lds(L_MISC + M_OC * 4, 16);
        put_wo(s, wo);
        lds_inc(M_WP);
    }
    if (s == 0) poll32(c, WS_FO + (1 * 8 + c.qr) * 32 * 128, M_OF, 2);
    bar();
    if (s == 1) { stats_exchange<1>(c); get_sel<1>(c); }
    STAMP(25);
    ffn_chain<1, 2>(c, s, round, wb);
    STAMP(26);
}

__global__ void __launch_bounds__(768, 3)
fused_kernel(const u16* x0, const u16* wqkv, const u16* wo, const u16* wr, const u16* wg, const u16* wu, const u16* wd, u16* out, char* ws) {
    Ctx c;
    { u64 pc; asm volatile("s_getpc_b64 %0" : "=s"(pc)); c.pc = (const char*)(pc & ~(u64)255); }
    c.x0 = x0; c.wqkv = wqkv; c.wo = wo; c.wr = wr; c.wg = wg; c.wu = wu; c.wd = wd; c.out = out; c.ws = ws;
    c.rs = __builtin_amdgcn_make_buffer_rsrc(ws, 0, 32u << 20, 0x00020000);
    c.bid = blockIdx.x; c.w = rfl(threadIdx.x >> 6);
    { const int x = c.bid & 7, sl = c.bid >> 3; c.qr = 2 * (x >> 1) + (sl & 1); c.h = 16 * (x & 1) + (sl >> 1); }
    STAMP(0);
    c.epv = ldf32(ws, WS_EPOCH); c.ep = 0;
    if (c.w >= 8) { side_program(c); }
    else {
        u32x4 xk[2] = {};
        qkv_phase<0>(c);
        STAMP(4);
        attn_phase<0>(c);
        oproj_phase<0>(c, xk);
        qkv_phase<1>(c);
        attn_phase<1>(c);
        oproj_phase<1>(c, xk);
        final_phase(c, xk);
    }
    STAMP_DUMP();
    if (c.bid == 112 && c.w == 8 && fresh_lane() == 0) stf32(ws, WS_EPOCH, c.ep);
}

extern "C" void kernel_launch(void* const* d_in, const int* in_sizes, int n_in, void* d_out, int out_size, void* d_ws, size_t ws_size, hipStream_t stream) {
    (void)in_sizes; (void)n_in; (void)out_size; (void)ws_size;
    (void)hipFuncSetAttribute((const void*)fused_kernel, hipFuncAttributeMaxDynamicSharedMemorySize, LDS_BYTES);
    fused_kernel<<<256, 768, LDS_BYTES, stream>>>((const u16*)d_in[0], (const u16*)d_in[1], (const u16*)d_in[2], (const u16*)d_in[4],
                                                  (const u16*)d_in[5], (const u16*)d_in[6], (const u16*)d_in[7], (u16*)d_out, (char*)d_ws);
}
```
